```python
import torch

def _softmax(x, axis=-1):
    return torch.softmax(x, dim=axis)

def _gelu(x):
    return torch.nn.functional.gelu(x, approximate='tanh')

def _silu(x):
    return torch.nn.functional.silu(x)

def _sigmoid(x):
    return torch.sigmoid(x)

def _layernorm(x, axis=-1, eps=1e-05):
    m = x.mean(dim=axis, keepdim=True)
    v = x.var(dim=axis, keepdim=True, unbiased=False)
    return (x - m) / torch.sqrt(v + eps)

def _scatter_add(tgt, idx, src, axis=0):
    return tgt.clone().index_add_(axis, idx, src)

def _segment_sum(src, seg, n):
    out = torch.zeros((n,) + src.shape[1:], dtype=src.dtype)
    return out.index_add_(0, seg, src)

def _pad(x, pw):
    flat = tuple((v for pair in reversed(pw) for v in pair))
    return torch.nn.functional.pad(x, flat)
kernel_name = 'standalone__molecular_angular_painn__graph'

def setup_inputs():

    _w = lambda a: (lambda t: t.to(torch.bfloat16) if t.is_floating_point() else t)(torch.as_tensor(a))
    N, deg, D = (12500, 32, 128)
    n_rbf = max(4, 4)
    E = N * deg
    cutoff = 5.0
    freqs = (torch.arange(1, n_rbf + 1) * torch.pi / cutoff).to(float)
    return {'sv': _w((lambda *s: torch.randn(*s, dtype=torch.bfloat16) * 0.4)(N, 4, D) * 0.3), 'pos': _w((lambda *s: torch.randn(*s, dtype=torch.bfloat16) * 3.2)(N, 3) * 1.2), 'src': _w((lambda lo, hi, *s: torch.randint(lo, hi, s))(0, N, E).to(torch.int64)), 'dst': _w((lambda lo, hi, *s: torch.randint(lo, hi, s))(0, N, E).to(torch.int64)), 'freqs': _w(freqs), 'w_phi1': _w((lambda *s: torch.randn(*s, dtype=torch.bfloat16) * 3.2)(D, D) / D ** 0.5), 'w_phi2': _w((lambda *s: torch.randn(*s, dtype=torch.bfloat16) * 3.2)(D, 3 * D) / D ** 0.5), 'w_rbf': _w((lambda *s: torch.randn(*s, dtype=torch.bfloat16) * 3.2)(n_rbf, 3 * D) / n_rbf ** 0.5)}

def reference(sv, pos, src, dst, freqs, w_phi1, w_phi2, w_rbf):
    N, _, D = sv.shape
    E = src.shape[0]
    PI, cutoff = (3.141592653589793, 5.0)
    s, v = (sv[:, 0, :].clone(), sv[:, 1:, :].clone())
    r_vec = pos[src] - pos[dst]
    d = torch.sqrt((r_vec * r_vec).sum(-1) + 1e-08)
    r_hat = r_vec / d[:, None]
    rbf = torch.sin(d[:, None] * freqs[None, :]) / d[:, None]
    fcut = 0.5 * (torch.cos(PI * d / cutoff) + 1.0)
    fcut = torch.where(d < cutoff, fcut, 0.0 * d)
    Wf = rbf @ w_rbf * fcut[:, None]
    phi = (_silu(s @ w_phi1) @ w_phi2)[src]
    m = phi * Wf
    m_s, m_vv, m_vr = (m[:, :D].clone(), m[:, D:2 * D].clone(), m[:, 2 * D:].clone())
    ds = _segment_sum(m_s, dst, N)
    dv_e = m_vv[:, None, :] * v[src] + m_vr[:, None, :] * r_hat[:, :, None]
    dv = _segment_sum(dv_e.reshape(E, 3 * D), dst, N).reshape(N, 3, D)
    return sv + torch.concatenate([ds[:, None, :], dv], axis=1)
```

```cpp
#include <hip/hip_runtime.h>
#include <hip/hip_bf16.h>
#include <stdint.h>

typedef unsigned short u16;
__device__ __forceinline__ float bf2f(u16 x){ return __uint_as_float(((unsigned)x)<<16); }
__device__ __forceinline__ u16 f2bf(float f){ __hip_bfloat16 b = __float2bfloat16(f); return *(u16*)&b; }
__device__ __forceinline__ float rbf16(float f){ return bf2f(f2bf(f)); }

__global__ void k_mlp(const u16* sv, const u16* w1, const u16* w2, u16* phi, int N){
  __shared__ float s[128]; __shared__ float h[128];
  int n = blockIdx.x; int t = threadIdx.x;
  s[t] = bf2f(sv[(size_t)n*512 + t]);
  __syncthreads();
  float acc = 0.f;
  for (int k=0;k<128;k++) acc += s[k]*bf2f(w1[k*128+t]);
  acc = rbf16(acc);
  float sl = acc/(1.f+expf(-acc));
  h[t] = rbf16(sl);
  __syncthreads();
  for (int g=0; g<3; g++){
    float a=0.f;
    for (int k=0;k<128;k++) a += h[k]*bf2f(w2[k*384+g*128+t]);
    phi[(size_t)n*384+g*128+t] = f2bf(a);
  }
}

__global__ void k_edge(const u16* sv, const u16* pos, const int* src, const int* dst, const u16* freqs,
                       const u16* wr, const u16* phi, float* acc, int E){
  int e = blockIdx.x; int t = threadIdx.x;
  int s = src[e], d = dst[e];
  float r[3]; float ss=0.f;
  for (int x=0;x<3;x++){ r[x] = rbf16(bf2f(pos[s*3+x]) - bf2f(pos[d*3+x])); ss += rbf16(r[x]*r[x]); }
  ss = rbf16(ss); ss = rbf16(ss + 1e-8f);
  float dd = rbf16(sqrtf(ss));
  if (dd >= 5.0f) return;
  float fc = rbf16(3.141592653589793f*dd); fc = rbf16(fc/5.0f); fc = rbf16(cosf(fc)); fc = rbf16(fc+1.0f); fc = rbf16(0.5f*fc);
  float rb[4];
  for (int k=0;k<4;k++){ float a = rbf16(dd*bf2f(freqs[k])); a = rbf16(sinf(a)); rb[k] = rbf16(a/dd);}
  float rh[3]; for (int x=0;x<3;x++) rh[x] = rbf16(r[x]/dd);
  float wf[3];
  for (int g=0;g<3;g++){ float a=0.f; for(int k=0;k<4;k++) a += rb[k]*bf2f(wr[k*384+g*128+t]); wf[g] = rbf16(rbf16(a)*fc); }
  float ms = rbf16(bf2f(phi[(size_t)s*384+t])*wf[0]);
  float mvv = rbf16(bf2f(phi[(size_t)s*384+128+t])*wf[1]);
  float mvr = rbf16(bf2f(phi[(size_t)s*384+256+t])*wf[2]);
  atomicAdd(&acc[(size_t)d*512+t], ms);
  for (int x=0;x<3;x++){
    float v = bf2f(sv[(size_t)s*512+128+x*128+t]);
    float dv = rbf16(rbf16(mvv*v) + rbf16(mvr*rh[x]));
    atomicAdd(&acc[(size_t)d*512+128+x*128+t], dv);
  }
}
__global__ void k_fin(const u16* sv, const float* acc, u16* out, int n){
  int i = blockIdx.x*256+threadIdx.x; if (i<n) out[i] = f2bf(bf2f(sv[i]) + rbf16(acc[i]));
}

extern "C" void kernel_launch(void* const* d_in, const int* in_sizes, int n_in,
                              void* d_out, int out_size, void* d_ws, size_t ws_size,
                              hipStream_t stream) {
  const u16* sv = (const u16*)d_in[0]; const u16* pos=(const u16*)d_in[1];
  const int* src=(const int*)d_in[2]; const int* dst=(const int*)d_in[3];
  const u16* freqs=(const u16*)d_in[4]; const u16* w1=(const u16*)d_in[5]; const u16* w2=(const u16*)d_in[6]; const u16* wr=(const u16*)d_in[7];
  int N = in_sizes[0]/512; int E = in_sizes[2];
  float* acc = (float*)d_ws; u16* phi = (u16*)((char*)d_ws + (size_t)N*512*4);
  hipMemsetAsync(acc, 0, (size_t)N*512*4, stream);
  k_mlp<<<N,128,0,stream>>>(sv,w1,w2,phi,N);
  k_edge<<<E,128,0,stream>>>(sv,pos,src,dst,freqs,wr,phi,acc,E);
  k_fin<<<(out_size+255)/256,256,0,stream>>>(sv,acc,(u16*)d_out,out_size);
}
```

```cpp
#include <hip/hip_runtime.h>
#include <stdint.h>

typedef unsigned short u16;
typedef unsigned int u32;
typedef float f2 __attribute__((ext_vector_type(2)));
typedef float f4 __attribute__((ext_vector_type(4)));
typedef u32 u2 __attribute__((ext_vector_type(2)));
typedef u32 u4 __attribute__((ext_vector_type(4)));
typedef short s4 __attribute__((ext_vector_type(4)));
typedef short s8 __attribute__((ext_vector_type(8)));
typedef __bf16 bf2 __attribute__((ext_vector_type(2)));

#define DI __device__ __forceinline__
#if defined(__HIP_DEVICE_COMPILE__)
#define NOPK __attribute__((target("no-packed-fp32-ops")))
#else
#define NOPK
#endif
#define TS(k, i)
#define TSW(k, i, w)
#define TSDRAIN

#define CSPLIT 2
#define JGROUPS 64
constexpr int CS = CSPLIT;
constexpr int NQ = 8 / CS;
constexpr int JG = JGROUPS;
constexpr int NCELL = NQ * JG;
constexpr int CHUNK = 4096;
#define RTILES 5
constexpr int RT = RTILES;
constexpr int MPL = (20480 / JG + 63) / 64;
constexpr int MMAX = MPL * 64;
constexpr int PMAX = 256;

DI float lo16(u32 x) { return __uint_as_float(x << 16); }
DI float hi16(u32 x) { return __uint_as_float(x & 0xffff0000u); }
DI u32 pkbf(float a, float b) { f2 f = {a, b}; bf2 v = __builtin_convertvector(f, bf2); return __builtin_bit_cast(u32, v); }
DI float rbf(float a) { return lo16(pkbf(a, 0.f)); }

template <int CTRL, int RMASK> DI u32 dpp_mov(u32 x) { return (u32)__builtin_amdgcn_update_dpp(0, (int)x, CTRL, RMASK, 0xf, false); }
DI u32 wave_scan(u32 x) {
  x += dpp_mov<0x111, 0xf>(x);
  x += dpp_mov<0x112, 0xf>(x);
  x += dpp_mov<0x114, 0xf>(x);
  x += dpp_mov<0x118, 0xf>(x);
  x += dpp_mov<0x142, 0xa>(x);
  x += dpp_mov<0x143, 0xc>(x);
  return x;
}
template <int CTRL, int RMASK> DI float dpp_addf(float x) { return x + __uint_as_float(dpp_mov<CTRL, RMASK>(__float_as_uint(x))); }
DI float row_scanf(float x) {
  x = dpp_addf<0x111, 0xf>(x); x = dpp_addf<0x112, 0xf>(x); x = dpp_addf<0x114, 0xf>(x); x = dpp_addf<0x118, 0xf>(x);
  return x;
}
DI float wave_scanf(float x) {
  x = row_scanf(x); x = dpp_addf<0x142, 0xa>(x); x = dpp_addf<0x143, 0xc>(x);
  return x;
}
DI float rdlanef(float x, int l) { return __uint_as_float(__builtin_amdgcn_readlane(__float_as_uint(x), l)); }
DI u32 quadrant(float x, float y, float z, float sx, float sy, float sz) {
  return (x > sx ? 1u : 0u) | (y > sy ? 2u : 0u) | ((CS == 1 && z > sz) ? 4u : 0u);
}

#define STORE_MODE 0
DI void st32(void* p, u32 v) {
  *(u32*)p = v;
}

template <int NPR>
DI void filter_role(char* lds, const u16* __restrict__ pos, const int* __restrict__ src, const int* __restrict__ dst,
                    u4* __restrict__ rec, u32* __restrict__ desc, float* __restrict__ split, int N, int E, int P, u32 magicM) {
  const int tid = threadIdx.x, lane = tid & 63, wv = __builtin_amdgcn_readfirstlane(tid >> 6), p = blockIdx.x;
  u32* hist = (u32*)lds;
  f4* red = (f4*)(lds + NCELL * 4);
  u2* tab = (u2*)(lds + NCELL * 4 + 256);
  TS(0, 0);
  const int e0 = p * CHUNK + tid * 4;
  const int npair = N >> 1;
  const char* posb = (const char*)pos;
  u32 sm0, sm1, sm2;
  { const u32* t = (const u32*)(posb + (u32)(12 * (int)(((unsigned)tid * (unsigned)npair) >> 10))); sm0 = t[0]; sm1 = t[1]; sm2 = t[2]; }
  u32 pw[NPR][3];
#pragma unroll
  for (int r = 0; r < NPR; r++) {
    const u32* t = (const u32*)(posb + (u32)(12 * min(tid + r * 1024, npair - 1)));
    pw[r][0] = t[0]; pw[r][1] = t[1]; pw[r][2] = t[2];
  }
  int sI[4], dI[4];
  if ((E & 3) == 0) {
    const int ec = min(e0, E - 4);
    int4 a = *(const int4*)(src + ec), b = *(const int4*)(dst + ec);
    sI[0] = a.x; sI[1] = a.y; sI[2] = a.z; sI[3] = a.w;
    dI[0] = b.x; dI[1] = b.y; dI[2] = b.z; dI[3] = b.w;
  } else {
#pragma unroll
    for (int u = 0; u < 4; u++) { const int ec = min(e0 + u, E - 1); sI[u] = src[ec]; dI[u] = dst[ec]; }
  }
  if (tid < NCELL) hist[tid] = 0;
  {
    float sx = wave_scanf(lo16(sm0) + hi16(sm1)), sy = wave_scanf(hi16(sm0) + lo16(sm2));
    float sz = CS == 1 ? wave_scanf(lo16(sm1) + hi16(sm2)) : 0.f;
    if (lane == 63) { f4 t = {sx, sy, sz, 0.f}; red[wv] = t; }
  }
#pragma unroll
  for (int r = 0; r < NPR; r++) {
    const int i = tid + r * 1024;
    if (i < npair) {
      u4 t;
      t.x = pw[r][0]; t.y = pw[r][1] & 0xffffu;
      t.z = __builtin_amdgcn_alignbit(pw[r][2], pw[r][1], 16); t.w = pw[r][2] >> 16;
      *(u4*)(tab + 2 * i) = t;
    }
  }
  if ((N & 1) && tid == 0) {
    const u32 n0 = N - 1;
    u2 t = {(u32)pos[3 * n0] | ((u32)pos[3 * n0 + 1] << 16), (u32)pos[3 * n0 + 2]};
    tab[n0] = t;
  }
  TS(0, 1);
  __syncthreads();
  TS(0, 2);
  float sx, sy, sz = 0.f;
  {
    f4 t = red[lane & 15];
    sx = rdlanef(row_scanf(t.x), 15) * (1.f / 2048.f); sy = rdlanef(row_scanf(t.y), 15) * (1.f / 2048.f);
    if (CS == 1) sz = rdlanef(row_scanf(t.z), 15) * (1.f / 2048.f);
  }
  if (p == 0 && tid == 0) { split[0] = sx; split[1] = sy; split[2] = sz; }
  u32 rk[4], rxy[4], rz[4];
  u2 ta[4], tb[4];
#pragma unroll
  for (int u = 0; u < 4; u++) { ta[u] = tab[sI[u]]; tb[u] = tab[dI[u]]; }
#pragma unroll
  for (int u = 0; u < 4; u++) {
    const u2 a = ta[u], b = tb[u];
    const float bx = lo16(b.x), by = hi16(b.x);
    const float rx = lo16(a.x) - bx, ry = hi16(a.x) - by, rzz = lo16(a.y) - lo16(b.y);
    const float d2 = rx * rx + ry * ry + rzz * rzz;
    const bool cand = (e0 + u < E) && !(d2 >= 25.25f && d2 <= 3.0e38f);
    rk[u] = 0xffffffffu;
    if (cand) {
      const u32 cell = quadrant(bx, by, lo16(b.y), sx, sy, sz) * JG + __umulhi((u32)dI[u], magicM);
      rk[u] = atomicAdd(&hist[cell], 1u) | (cell << 16);
    }
    rxy[u] = pkbf(rx, ry); rz[u] = pkbf(rzz, 0.f);
  }
  __syncthreads();
  TS(0, 3);
  if (wv == 0) {
    constexpr int CPL = NCELL / 64;
    u32 c[CPL], o[CPL], s = 0;
#pragma unroll
    for (int i = 0; i < CPL; i += 4) { u4 t = *(const u4*)(hist + lane * CPL + i); c[i] = t.x; c[i + 1] = t.y; c[i + 2] = t.z; c[i + 3] = t.w; }
#pragma unroll
    for (int i = 0; i < CPL; i++) s += c[i];
    u32 ex = wave_scan(s) - s;
#pragma unroll
    for (int i = 0; i < CPL; i++) { o[i] = ex; ex += c[i]; }
#pragma unroll
    for (int i = 0; i < CPL; i += 4) { u4 t = {o[i], o[i + 1], o[i + 2], o[i + 3]}; *(u4*)(hist + lane * CPL + i) = t; }
#pragma unroll
    for (int i = 0; i < CPL; i++) desc[(lane * CPL + i) * P + p] = o[i] | (c[i] << 16);
  }
  __syncthreads();
  TS(0, 4);
#pragma unroll
  for (int u = 0; u < 4; u++) {
    if (rk[u] != 0xffffffffu) {
      const u32 idx = hist[rk[u] >> 16] + (rk[u] & 0xffffu);
      u4 t = {(u32)sI[u], (u32)dI[u], rxy[u], rz[u]};
      rec[(size_t)p * CHUNK + idx] = t;
    }
  }
  TS(0, 5);
  TSDRAIN;
  TS(0, 6);
}

constexpr int HP = 272;
DI s8 as_s8(u4 x) { return __builtin_bit_cast(s8, x); }

DI void mlp_role(char* lds, const u16* __restrict__ sv, const u16* __restrict__ w1, const u16* __restrict__ w2,
                 u16* __restrict__ phi, int N, int tile) {
  const int tid = threadIdx.x, lane = tid & 63, wv = __builtin_amdgcn_readfirstlane(tid >> 6);
  const int g = lane >> 4, c = lane & 15;
  const int n0 = tile * (RT * 16);
  TS(0, 0);
  char* sS = lds;
  char* sH = lds + RT * 16 * HP;
  const char* svb = (const char*)sv;
  u4 sreg[2];
#pragma unroll
  for (int r = 0; r < 2; r++) {
    const int i = min(tid + r * 1024, RT * 256 - 1), row = i >> 4, ch = i & 15;
    sreg[r] = *(const u4*)(svb + (u32)(min(n0 + row, N - 1) * 1024 + ch * 16));
  }
  const char* wp = (const char*)((wv < 4) ? (w1 + 32 * wv) : (w2 + 32 * (wv - 4)));
  const int wld = (wv < 4) ? 256 : 768;
  u32 L[32];
  {
    const u32 wo = (u32)(8 * g * wld + 4 * c);
#pragma unroll
    for (int s = 0; s < 4; s++)
#pragma unroll
      for (int j = 0; j < 8; j++) L[s * 8 + j] = *(const u32*)(wp + (size_t)((32 * s + j) * wld) + wo);
  }
  u32 warm;
  {
    const int i = min(tid, RT * 16 * 6 - 1), row = i / 6, ln = i - row * 6;
    warm = *(const u32*)(svb + (u32)(min(n0 + row, N - 1) * 1024 + 256 + ln * 128));
  }
#pragma unroll
  for (int r = 0; r < 2; r++) {
    const int i = tid + r * 1024, row = i >> 4, ch = i & 15;
    if (row < RT * 16) *(u4*)(sS + row * HP + ch * 16) = sreg[r];
  }
  TS(0, 1);
  __syncthreads();
  TS(0, 2);
#pragma unroll
  for (int i = 0; i < 32; i += 8)
    asm volatile("" : "+v"(L[i]), "+v"(L[i + 1]), "+v"(L[i + 2]), "+v"(L[i + 3]), "+v"(L[i + 4]), "+v"(L[i + 5]), "+v"(L[i + 6]), "+v"(L[i + 7]));
  u4 B[2][4];
#pragma unroll
  for (int s = 0; s < 4; s++)
#pragma unroll
    for (int i = 0; i < 4; i++) {
      B[0][s][i] = __builtin_amdgcn_perm(L[s * 8 + 2 * i + 1], L[s * 8 + 2 * i], 0x05040100u);
      B[1][s][i] = __builtin_amdgcn_perm(L[s * 8 + 2 * i + 1], L[s * 8 + 2 * i], 0x07060302u);
    }
  if (wv < 4) {
    f4 a0[RT], a1[RT];
#pragma unroll
    for (int rt = 0; rt < RT; rt++) { f4 z = {0, 0, 0, 0}; a0[rt] = z; a1[rt] = z; }
#pragma unroll
    for (int s = 0; s < 4; s++)
#pragma unroll
      for (int rt = 0; rt < RT; rt++) {
        u4 A = *(const u4*)(sS + (rt * 16 + c) * HP + (32 * s + 8 * g) * 2);
        a0[rt] = __builtin_amdgcn_mfma_f32_16x16x32_bf16(as_s8(A), as_s8(B[0][s]), a0[rt], 0, 0, 0);
        a1[rt] = __builtin_amdgcn_mfma_f32_16x16x32_bf16(as_s8(A), as_s8(B[1][s]), a1[rt], 0, 0, 0);
      }
#pragma unroll
    for (int rt = 0; rt < RT; rt++)
#pragma unroll
      for (int r = 0; r < 4; r++) {
        const u32 xr = pkbf(a0[rt][r], a1[rt][r]);
        const float x0 = lo16(xr), x1 = hi16(xr);
        const float h0 = x0 * __builtin_amdgcn_rcpf(1.f + __builtin_amdgcn_exp2f(x0 * -1.4426950408889634f));
        const float h1 = x1 * __builtin_amdgcn_rcpf(1.f + __builtin_amdgcn_exp2f(x1 * -1.4426950408889634f));
        *(u32*)(sH + (rt * 16 + 4 * g + r) * HP + (32 * wv + 2 * c) * 2) = pkbf(h0, h1);
      }
  }
  TS(0, 3);
  __syncthreads();
  TSW(0, 4, 4);
  if (wv >= 4) {
    char* ob = (char*)phi + (size_t)n0 * 768 + (32 * (wv - 4)) * 2;
    const u32 oo = (u32)(4 * g * 768 + 4 * c);
    const bool full = n0 + RT * 16 <= N;
#pragma unroll
    for (int rt = 0; rt < RT; rt++) {
      f4 a0 = {0, 0, 0, 0}, a1 = {0, 0, 0, 0};
#pragma unroll
      for (int s = 0; s < 4; s++) {
        u4 A = *(const u4*)(sH + (rt * 16 + c) * HP + (32 * s + 8 * g) * 2);
        a0 = __builtin_amdgcn_mfma_f32_16x16x32_bf16(as_s8(A), as_s8(B[0][s]), a0, 0, 0, 0);
        a1 = __builtin_amdgcn_mfma_f32_16x16x32_bf16(as_s8(A), as_s8(B[1][s]), a1, 0, 0, 0);
      }
      if (full) {
#pragma unroll
        for (int r = 0; r < 4; r++) st32(ob + (size_t)((rt * 16 + r) * 768) + oo, pkbf(a0[r], a1[r]));
      } else {
#pragma unroll
        for (int r = 0; r < 4; r++)
          if (n0 + rt * 16 + 4 * g + r < N) st32(ob + (size_t)((rt * 16 + r) * 768) + oo, pkbf(a0[r], a1[r]));
      }
    }
  }
  TSW(0, 5, 4);
  TSDRAIN;
  TSW(0, 6, 4);
  asm volatile("" ::"v"(warm));
}

template <int NPR>
__global__ __launch_bounds__(1024) void k_prep(const u16* __restrict__ sv, const u16* __restrict__ pos, const int* __restrict__ src,
                                               const int* __restrict__ dst, const u16* __restrict__ w1, const u16* __restrict__ w2,
                                               u16* __restrict__ phi, u4* __restrict__ rec, u32* __restrict__ desc,
                                               float* __restrict__ split, int N, int E, int P, u32 magicM) {
  extern __shared__ __attribute__((aligned(16))) char lds[];
  if ((int)blockIdx.x < P) filter_role<NPR>(lds, pos, src, dst, rec, desc, split, N, E, P, magicM);
  else mlp_role(lds, sv, w1, w2, phi, N, blockIdx.x - P);
}

#define K2W 8
#define K2OCC 4
constexpr int NW = K2W;
constexpr int NT = NW * 64;
constexpr int CH = NT;
constexpr int SLOTS = CH + (CS == 2 ? MMAX : 0);
constexpr int LPC = 64 / CS;
constexpr int L_PA = 0;
constexpr int L_PB = L_PA + SLOTS * 16;
constexpr int L_RAW = L_PB;
constexpr int L_CNT = L_PB + SLOTS * 16;
constexpr int L_START = L_CNT + MMAX * 4;
constexpr int L_INFO = L_START + MMAX * 4;
constexpr int L_MISC = L_INFO + SLOTS * 4;
constexpr int L_W = L_MISC + 256;
constexpr int L_TOTAL = L_W + 6 * LPC * 8;

struct TrueT { static constexpr bool value = true; };
struct FalseT { static constexpr bool value = false; };
DI f4 mfma444(u2 a, u2 b, f4 c) {
  return __builtin_amdgcn_mfma_f32_4x4x4bf16_1k(__builtin_bit_cast(s4, a), __builtin_bit_cast(s4, b), c, 0, 0, 0);
}

DI void agg_range(const char* lds, const char* phb, const char* svb, const char* resb, char* out, u32 lofs, u32 nb, int lane,
                  int itb, int ite) {
  const u32* info = (const u32*)(lds + L_INFO);
  const u4* pa = (const u4*)(lds + L_PA);
  const u4* pb = (const u4*)(lds + L_PB);
  const int half = CS == 2 ? lane >> 5 : 0, lp = lane & (LPC - 1);
  u2 wt[3][2];
#pragma unroll
  for (int g = 0; g < 3; g++)
#pragma unroll
    for (int t = 0; t < 2; t++) wt[g][t] = *(const u2*)(lds + L_W + ((g * 2 + t) * LPC + lp) * 8);
  u2 ident;
  ident.x = (lane & 3) == 0 ? 0x00003f80u : ((lane & 3) == 1 ? 0x3f800000u : 0u);
  ident.y = (lane & 3) == 2 ? 0x00003f80u : ((lane & 3) == 3 ? 0x3f800000u : 0u);
  const f4 zero = {0, 0, 0, 0};
  float acc[4][2];
#pragma unroll
  for (int r = 0; r < 4; r++) acc[r][0] = acc[r][1] = 0.f;
  const u32 olane = (CS == 2 ? half * 512u : 0u) + lofs;
  for (int it = itb; it < ite; it += 4) {
    const int iq = min(it + (lane & 3), ite - 1);
    const u32 myinf = info[iq];
    const u4 am = pa[CS * iq + half];
    u2 b[4][3];
#pragma unroll
    for (int u = 0; u < 4; u++) {
      const int i = min(it + u, ite - 1);
      const u32 s = pb[CS * i + half].w;
      const u32 op = __umul24(s, 768u) + lofs, ov = (s << 10) + lofs;
      b[u][0].x = *(const u32*)(phb + (size_t)op);
      b[u][0].y = *(const u32*)(phb + (size_t)op + 256);
      b[u][1].x = *(const u32*)(phb + (size_t)op + 512);
      b[u][1].y = *(const u32*)(svb + (size_t)ov + 256);
      b[u][2].x = *(const u32*)(svb + (size_t)ov + 512);
      b[u][2].y = *(const u32*)(svb + (size_t)ov + 768);
    }
    u32 inf[4], q[4][4 / CS];
#pragma unroll
    for (int u = 0; u < 4; u++) {
      inf[u] = __builtin_amdgcn_readlane(myinf, u);
      const char* rp = resb + (size_t)((nb + (inf[u] & 0xffffu)) * 1024u);
#pragma unroll
      for (int r = 0; r < 4 / CS; r++) q[u][r] = *(const u32*)(rp + (size_t)olane + r * 256);
    }
    f4 wf[3][2];
#pragma unroll
    for (int g = 0; g < 3; g++)
#pragma unroll
      for (int t = 0; t < 2; t++) {
        u2 ah = {am.x, am.y}, al = {am.z, am.w};
        wf[g][t] = mfma444(al, wt[g][t], mfma444(ah, wt[g][t], zero));
      }
#pragma unroll
    for (int u = 0; u < 4; u++) {
      if (it + u < ite) {
        const u4 rb = pb[CS * (it + u) + half];
        const f4 U0 = mfma444(ident, b[u][0], zero), U1 = mfma444(ident, b[u][1], zero), U2 = mfma444(ident, b[u][2], zero);
        const float r0 = __uint_as_float(rb.x), r1 = __uint_as_float(rb.y), r2 = __uint_as_float(rb.z);
        acc[0][0] = __builtin_fmaf(U0[0], wf[0][0][u], acc[0][0]);
        acc[0][1] = __builtin_fmaf(U0[1], wf[0][1][u], acc[0][1]);
        const float mv0 = U0[2] * wf[1][0][u], mv1 = U0[3] * wf[1][1][u];
        const float mr0 = U1[0] * wf[2][0][u], mr1 = U1[1] * wf[2][1][u];
        acc[1][0] = __builtin_fmaf(mv0, U1[2], acc[1][0]); acc[1][1] = __builtin_fmaf(mv1, U1[3], acc[1][1]);
        acc[2][0] = __builtin_fmaf(mv0, U2[0], acc[2][0]); acc[2][1] = __builtin_fmaf(mv1, U2[1], acc[2][1]);
        acc[3][0] = __builtin_fmaf(mv0, U2[2], acc[3][0]); acc[3][1] = __builtin_fmaf(mv1, U2[3], acc[3][1]);
        acc[1][0] = __builtin_fmaf(mr0, r0, acc[1][0]); acc[1][1] = __builtin_fmaf(mr1, r0, acc[1][1]);
        acc[2][0] = __builtin_fmaf(mr0, r1, acc[2][0]); acc[2][1] = __builtin_fmaf(mr1, r1, acc[2][1]);
        acc[3][0] = __builtin_fmaf(mr0, r2, acc[3][0]); acc[3][1] = __builtin_fmaf(mr1, r2, acc[3][1]);
        if (inf[u] & 0x10000u) {
          char* on = out + (size_t)((nb + (inf[u] & 0xffffu)) * 1024u);
          if (CS == 2) {
            float o[2][2];
#pragma unroll
            for (int r = 0; r < 2; r++)
#pragma unroll
              for (int cc = 0; cc < 2; cc++) {
                auto x = __builtin_amdgcn_permlane32_swap(__float_as_uint(acc[r][cc]), __float_as_uint(acc[r + 2][cc]), false, false);
                o[r][cc] = __uint_as_float(x[0]) + __uint_as_float(x[1]);
              }
#pragma unroll
            for (int r = 0; r < 2; r++) *(u32*)(on + (size_t)olane + r * 256) = pkbf(lo16(q[u][r]) + o[r][0], hi16(q[u][r]) + o[r][1]);
          } else {
#pragma unroll
            for (int r = 0; r < 4; r++) *(u32*)(on + (size_t)olane + r * 256) = pkbf(lo16(q[u][r % (4 / CS)]) + acc[r][0], hi16(q[u][r % (4 / CS)]) + acc[r][1]);
          }
#pragma unroll
          for (int r = 0; r < 4; r++) acc[r][0] = acc[r][1] = 0.f;
        }
      }
    }
  }
}

NOPK __global__ __launch_bounds__(NT, K2OCC) void k_agg(const u16* __restrict__ sv, const u16* __restrict__ pos, const u16* __restrict__ freqs,
                                                        const u16* __restrict__ wr, const u16* __restrict__ phi, const u4* __restrict__ rec,
                                                        const u32* __restrict__ desc, const float* __restrict__ split,
                                                        u16* out, int N, int P, int M) {
  __shared__ __attribute__((aligned(16))) char lds[L_TOTAL];
  const int tid = threadIdx.x, lane = tid & 63, wv = __builtin_amdgcn_readfirstlane(tid >> 6);
  const int label = blockIdx.x & 7, q = CS == 2 ? label >> 1 : label, h = CS == 2 ? label & 1 : 0, j = blockIdx.x >> 3;
  const int cell = q * JG + j, nb = j * M;
  const int half = CS == 2 ? lane >> 5 : 0, lp = lane & (LPC - 1);
  u32* cnt = (u32*)(lds + L_CNT);
  u32* start = (u32*)(lds + L_START);
  u32* info = (u32*)(lds + L_INFO);
  u32* misc = (u32*)(lds + L_MISC);
  u4* raw = (u4*)(lds + L_RAW);
  u4* pa = (u4*)(lds + L_PA);
  u4* pb = (u4*)(lds + L_PB);
  const char* svb = (const char*)sv;
  const int npw = (P + 63) >> 6, nmw = (M + 63) >> 6;

  TS(1, 0);
  u32 dsc = 0, px = 0, py = 0, pz = 0;
  if (wv < npw) dsc = desc[cell * P + min(tid, P - 1)];
  const u32 nc = min(nb + tid, N - 1);
  if (wv < nmw) {
    const u16* pp = (const u16*)((const char*)pos + nc * 6u);
    px = pp[0]; py = pp[1]; if (CS == 1) pz = pp[2];
  }
  const float sx = split[0], sy = split[1], sz = CS == 1 ? split[2] : 0.f;
  u32 wk[4] = {0, 0, 0, 0};
  constexpr int NWE = 6 * LPC;
  const int we = min(tid, NWE - 1);
  if (wv < (NWE + 63) / 64) {
    const int gt = we / LPC, l = we % LPC;
    const u16* wp = wr + (gt >> 1) * 128 + h * 64 + 2 * l + (gt & 1);
#pragma unroll
    for (int k = 0; k < 4; k++) wk[k] = wp[k * 384];
  }
  static_assert(NWE <= NT, "W staging");
  const u32 fq01 = *(const u32*)freqs, fq23 = *(const u32*)(freqs + 2);
  for (int i = tid; i < MMAX; i += NT) cnt[i] = 0;

  u32 rc = 0, rs = 0;
  const u4* myrec = rec;
  u4 pre[4];
  if (wv < npw) {
    rc = tid < P ? dsc >> 16 : 0u;
    myrec = rec + (size_t)min(tid, P - 1) * CHUNK + (rc ? (dsc & 0xffffu) : 0u);
#pragma unroll
    for (int k = 0; k < 4; k++) pre[k] = myrec[min((u32)k, rc ? rc - 1 : 0u)];
    rs = wave_scan(rc);
    if (lane == 63) misc[wv] = rs;
  }
  bool member = false;
  u32 touch[8 / CS];
#pragma unroll
  for (int i = 0; i < 8 / CS; i++) touch[i] = 0;
  if (wv < nmw) {
    member = tid < M && nb + tid < N && quadrant(lo16(px), lo16(py), lo16(pz), sx, sy, sz) == (u32)q;
    if (member) {
#pragma unroll
      for (int i = 0; i < 8 / CS; i++) touch[i] = *(const u32*)(svb + (nc * 1024u + (CS == 2 ? i * 256u + h * 128u : i * 128u)));
    }
  }
  if (tid < NWE) { u2 t = {wk[0] | (wk[1] << 16), wk[2] | (wk[3] << 16)}; *(u2*)(lds + L_W + tid * 8) = t; }
  TS(1, 1);
  __syncthreads();
  u32 roff = 0, nrec = 0;
  for (int i = 0; i < npw; i++) { const u32 b = misc[i]; nrec += b; if (i < wv) roff += b; }
  nrec = __builtin_amdgcn_readfirstlane(nrec);
  const u32 rpre = roff + rs - rc;
  TS(1, 2);
  const u32 lofs = h * 128 + lp * 4;

  auto process = [&](auto first_tag, u32 c0) __attribute__((always_inline)) {
    constexpr bool FIRST = decltype(first_tag)::value;
    bool act = false; u32 loc = 0, rnk = 0;
    u4 ca = {0, 0, 0, 0}, cb = {0, 0, 0, 0};
    if (c0 + wv * 64 < nrec) {
      const float frq[4] = {lo16(fq01), hi16(fq01), lo16(fq23), hi16(fq23)};
      const u4 t = raw[tid];
      loc = t.y - nb;
      const float rx = lo16(t.z), ry = hi16(t.z), rz = lo16(t.w);
      const u32 s01 = pkbf(rx * rx, ry * ry);
      float ss = lo16(s01) + hi16(s01) + rbf(rz * rz);
      ss = rbf(ss); ss = rbf(ss + 1e-8f);
      const float dd = rbf(__builtin_amdgcn_sqrtf(ss));
      act = c0 + tid < nrec && !(dd >= 5.0f && dd <= 3.0e38f);
      if (act) rnk = atomicAdd(&cnt[loc], 1u);
      const float id = __builtin_amdgcn_rcpf(dd);
      const u32 rh01 = pkbf(rx * id, ry * id);
      cb.x = rh01 << 16; cb.y = rh01 & 0xffff0000u; cb.z = pkbf(0.f, rz * id) & 0xffff0000u; cb.w = t.x;
      float fc = rbf(3.14159265358979f * dd);
      fc = rbf(fc * 0.2f);
      fc = rbf(__builtin_amdgcn_cosf(__builtin_amdgcn_fractf(fc * 0.15915494309189535f)));
      fc = 0.5f * rbf(fc + 1.0f);
      if (!(dd < 5.0f)) fc = 0.0f * dd;
      float a[4];
#pragma unroll
      for (int k = 0; k < 4; k += 2) {
        const u32 x = pkbf(dd * frq[k], dd * frq[k + 1]);
        const u32 y = pkbf(__builtin_amdgcn_sinf(__builtin_amdgcn_fractf(lo16(x) * 0.15915494309189535f)),
                           __builtin_amdgcn_sinf(__builtin_amdgcn_fractf(hi16(x) * 0.15915494309189535f)));
        const u32 z = pkbf(lo16(y) * id, hi16(y) * id);
        a[k] = lo16(z) * fc; a[k + 1] = hi16(z) * fc;
      }
      ca.x = pkbf(a[0], a[1]); ca.y = pkbf(a[2], a[3]);
      ca.z = pkbf(a[0] - lo16(ca.x), a[1] - hi16(ca.x)); ca.w = pkbf(a[2] - lo16(ca.y), a[3] - hi16(ca.y));
    }
    __syncthreads();
    TS(1, 4);
    if (wv == 0) {
      u32 it[MPL], sum = 0;
#pragma unroll
      for (int i = 0; i < MPL; i++) { const u32 cc = cnt[lane * MPL + i]; it[i] = CS == 2 ? (cc + 1) >> 1 : cc; sum += it[i]; }
      const u32 inc = wave_scan(sum);
      const u32 T = __builtin_amdgcn_readlane(inc, 63);
      if (lane <= NW) misc[16 + lane] = T;
      const float sc = (float)NW / (float)(T ? T : 1);
      u32 ex = inc - sum;
#pragma unroll
      for (int i = 0; i < MPL; i++) {
        start[lane * MPL + i] = ex;
        if (it[i]) atomicMin(&misc[16 + min((int)((float)ex * sc), NW - 1)], ex);
        ex += it[i];
      }
    }
    __syncthreads();
    if (act) {
      const u32 st = start[loc], sl = CS * st + rnk;
      pa[sl] = ca; pb[sl] = cb;
      const u32 cc = cnt[loc];
      if (CS == 2) {
        if (rnk + 1 == cc && (cc & 1)) { u4 z = {0, 0, 0, 0}; pa[sl + 1] = z; u4 t = {0, 0, 0, cb.w}; pb[sl + 1] = t; }
        if ((rnk & 1) == 0) info[st + (rnk >> 1)] = loc | ((rnk + 2 >= cc) ? 0x10000u : 0u);
      } else {
        info[sl] = loc | ((rnk + 1 == cc) ? 0x10000u : 0u);
      }
    }
    __syncthreads();
    TS(1, 5);
    u32 bv = misc[16 + min(lane, NW)];
    int itb = wv == 0 ? 0 : (int)__builtin_amdgcn_readlane(bv, wv), ite = (int)__builtin_amdgcn_readlane(bv, NW);
#pragma unroll
    for (int i = NW - 1; i >= 1; i--) { const int x = (int)__builtin_amdgcn_readlane(bv, i); if (i > wv) ite = min(ite, x); }
    itb = min(itb, ite);
    TS(1, 6);
    agg_range(lds, (const char*)phi, svb, FIRST ? svb : (const char*)out, (char*)out, lofs, nb, lane, itb, ite);
    TS(1, 7);
  };
  if (wv < npw) {
#pragma unroll
    for (int k = 0; k < 4; k++) if ((u32)k < rc && rpre + k < CH) raw[rpre + k] = pre[k];
    for (u32 k = 4; k < rc; k++) if (rpre + k < CH) raw[rpre + k] = myrec[k];
  }
  __syncthreads();
  TS(1, 3);
  process(TrueT(), 0u);
  if (wv < nmw) {
    unsigned long long em = __ballot(member && cnt[min(tid, MMAX - 1)] == 0);
    while (em) {
      const int l = __builtin_ctzll(em); em &= em - 1;
      const size_t go = (size_t)(nb + wv * 64 + l) * 1024 + (CS == 2 ? half * 512u : 0u) + lofs;
#pragma unroll
      for (int r = 0; r < 4 / CS; r++) *(u32*)((char*)out + go + r * 256) = *(const u32*)(svb + go + r * 256);
    }
  }
  for (u32 c0 = CH; c0 < nrec; c0 += CH) {
    __syncthreads();
    for (int i = tid; i < MMAX; i += NT) cnt[i] = 0;
    for (u32 k = 0; k < rc; k++) { u32 t = rpre + k - c0; if (rpre + k >= c0 && t < CH) raw[t] = myrec[k]; }
    __syncthreads();
    process(FalseT(), c0);
  }
  TS(1, 8);
  TSDRAIN;
  TS(1, 9);
#pragma unroll
  for (int i = 0; i < 8 / CS; i++) asm volatile("" ::"v"(touch[i]));
}

__global__ void fb_mlp(const u16* sv, const u16* w1, const u16* w2, u16* phi) {
  __shared__ float s[128], hh[128];
  int n = blockIdx.x, t = threadIdx.x;
  s[t] = lo16(sv[(size_t)n * 512 + t]);
  __syncthreads();
  float acc = 0.f;
  for (int k = 0; k < 128; k++) acc += s[k] * lo16(w1[k * 128 + t]);
  acc = rbf(acc);
  hh[t] = rbf(acc / (1.f + expf(-acc)));
  __syncthreads();
  for (int g = 0; g < 3; g++) {
    float a = 0.f;
    for (int k = 0; k < 128; k++) a += hh[k] * lo16(w2[k * 384 + g * 128 + t]);
    phi[(size_t)n * 384 + g * 128 + t] = (u16)pkbf(a, 0.f);
  }
}
__global__ void fb_edge(const u16* sv, const u16* pos, const int* src, const int* dst, const u16* freqs, const u16* wr,
                        const u16* phi, float* acc) {
  int e = blockIdx.x, t = threadIdx.x;
  size_t s = src[e], d = dst[e];
  float r[3], ss = 0.f;
  for (int x = 0; x < 3; x++) { r[x] = rbf(lo16(pos[s * 3 + x]) - lo16(pos[d * 3 + x])); ss += rbf(r[x] * r[x]); }
  ss = rbf(ss); ss = rbf(ss + 1e-8f);
  float dd = rbf(sqrtf(ss));
  if (dd >= 5.0f && dd <= 3.0e38f) return;
  float fc = rbf(3.14159265358979f * dd); fc = rbf(fc / 5.0f); fc = rbf(cosf(fc)); fc = rbf(fc + 1.0f); fc = rbf(0.5f * fc);
  if (!(dd < 5.0f)) fc = 0.0f * dd;
  float wf[3] = {0.f, 0.f, 0.f};
  for (int k = 0; k < 4; k++) {
    float a = rbf(rbf(sinf(rbf(dd * lo16(freqs[k])))) / dd) * fc;
    for (int g = 0; g < 3; g++) wf[g] += a * lo16(wr[k * 384 + g * 128 + t]);
  }
  atomicAdd(&acc[d * 512 + t], lo16(phi[s * 384 + t]) * wf[0]);
  float mv = lo16(phi[s * 384 + 128 + t]) * wf[1], mr = lo16(phi[s * 384 + 256 + t]) * wf[2];
  for (int x = 0; x < 3; x++) atomicAdd(&acc[d * 512 + 128 + x * 128 + t], mv * lo16(sv[s * 512 + 128 + x * 128 + t]) + mr * rbf(r[x] / dd));
}
__global__ void fb_fin(const u16* sv, const float* acc, u16* out, size_t n) {
  size_t i = blockIdx.x * (size_t)256 + threadIdx.x;
  if (i < n) out[i] = (u16)pkbf(lo16(sv[i]) + acc[i], 0.f);
}

extern "C" void kernel_launch(void* const* d_in, const int* in_sizes, int n_in, void* d_out, int out_size, void* d_ws,
                              size_t ws_size, hipStream_t stream) {
  const u16* sv = (const u16*)d_in[0];
  const u16* pos = (const u16*)d_in[1];
  const int* src = (const int*)d_in[2];
  const int* dst = (const int*)d_in[3];
  const u16* freqs = (const u16*)d_in[4];
  const u16* w1 = (const u16*)d_in[5];
  const u16* w2 = (const u16*)d_in[6];
  const u16* wr = (const u16*)d_in[7];
  u16* out = (u16*)d_out;
  const int N = in_sizes[0] / 512, E = in_sizes[2];
  const int P = (E + CHUNK - 1) / CHUNK;
  const int M = (N + JG - 1) / JG;
  char* ws = (char*)d_ws;
  u16* phi = (u16*)ws;
  size_t off = ((size_t)N * 768 + 255) & ~(size_t)255;
  if (N >= 2 && E >= 4 && M <= MMAX && M <= NT && N <= 20000 && P <= PMAX && P <= NT) {
    u4* rec = (u4*)(ws + off); off += (size_t)P * CHUNK * 16;
    u32* desc = (u32*)(ws + off); off += (((size_t)P * NCELL * 4) + 255) & ~(size_t)255;
    float* split = (float*)(ws + off);
    const u32 magicM = (u32)(0x100000000ull / (u32)M) + 1u;
    const int nmlp = (N + RT * 16 - 1) / (RT * 16);
    size_t lds1 = (size_t)((N + 1) & ~1) * 8 + NCELL * 4 + 256;
    const size_t ldsm = 2 * RT * 16 * HP;
    if (lds1 < ldsm) lds1 = ldsm;
    if (N <= 7 * 2048) {
      (void)hipFuncSetAttribute((const void*)k_prep<7>, hipFuncAttributeMaxDynamicSharedMemorySize, 160 * 1024);
      k_prep<7><<<P + nmlp, 1024, lds1, stream>>>(sv, pos, src, dst, w1, w2, phi, rec, desc, split, N, E, P, magicM);
    } else {
      (void)hipFuncSetAttribute((const void*)k_prep<10>, hipFuncAttributeMaxDynamicSharedMemorySize, 160 * 1024);
      k_prep<10><<<P + nmlp, 1024, lds1, stream>>>(sv, pos, src, dst, w1, w2, phi, rec, desc, split, N, E, P, magicM);
    }
    k_agg<<<8 * JG, NT, 0, stream>>>(sv, pos, freqs, wr, phi, rec, desc, split, out, N, P, M);
  } else {
    float* acc = (float*)(ws + off);
    (void)hipMemsetAsync(acc, 0, (size_t)N * 512 * 4, stream);
    fb_mlp<<<N, 128, 0, stream>>>(sv, w1, w2, phi);
    if (E > 0) fb_edge<<<E, 128, 0, stream>>>(sv, pos, src, dst, freqs, wr, phi, acc);
    fb_fin<<<(unsigned)(((size_t)N * 512 + 255) / 256), 256, 0, stream>>>(sv, acc, out, (size_t)N * 512);
  }
}
```

```cpp
#include <hip/hip_runtime.h>
#include <stdint.h>

typedef unsigned short u16;
typedef unsigned int u32;
typedef float f2 __attribute__((ext_vector_type(2)));
typedef float f4 __attribute__((ext_vector_type(4)));
typedef u32 u2 __attribute__((ext_vector_type(2)));
typedef u32 u4 __attribute__((ext_vector_type(4)));
typedef short s4 __attribute__((ext_vector_type(4)));
typedef short s8 __attribute__((ext_vector_type(8)));
typedef __bf16 bf2 __attribute__((ext_vector_type(2)));

#define DI __device__ __forceinline__
#if defined(__HIP_DEVICE_COMPILE__)
#define NOPK __attribute__((target("no-packed-fp32-ops")))
#else
#define NOPK
#endif
#define TS(k, i)
#define TSW(k, i, w)
#define TSDRAIN

#define CSPLIT 2
#define JGROUPS 64
constexpr int CS = CSPLIT;
constexpr int NQ = 8 / CS;
constexpr int JG = JGROUPS;
constexpr int NCELL = NQ * JG;
constexpr int CHUNK = 4096;
#define RTILES 5
constexpr int RT = RTILES;
constexpr int MPL = 4;
constexpr int MMAX = MPL * 64;
constexpr int PMAX = 128;
constexpr int SLOTK = 8;

DI float lo16(u32 x) { return __uint_as_float(x << 16); }
DI float hi16(u32 x) { return __uint_as_float(x & 0xffff0000u); }
DI u32 pkbf(float a, float b) { f2 f = {a, b}; bf2 v = __builtin_convertvector(f, bf2); return __builtin_bit_cast(u32, v); }
DI float rbf(float a) { return lo16(pkbf(a, 0.f)); }

DI void bar_lds() {
  asm volatile("s_waitcnt lgkmcnt(0)" ::: "memory");
  __builtin_amdgcn_s_barrier();
  asm volatile("" ::: "memory");
}
DI void bar_all() {
  asm volatile("s_waitcnt vmcnt(0) lgkmcnt(0)" ::: "memory");
  __builtin_amdgcn_s_barrier();
  asm volatile("" ::: "memory");
}
DI unsigned long long ballot64(bool p) { return __builtin_amdgcn_ballot_w64(p); }
typedef __amdgpu_buffer_rsrc_t rsrc_t;
DI rsrc_t mk_rsrc(const void* p, u32 bytes) { return __builtin_amdgcn_make_buffer_rsrc((void*)p, 0, bytes, 0x00020000); }
DI u32 bld(rsrc_t r, u32 vo, u32 so) { return __builtin_amdgcn_raw_buffer_load_b32(r, vo, so, 0); }
#define OUT_AUX 16
DI void bst(u32 v, rsrc_t r, u32 vo, u32 so) { __builtin_amdgcn_raw_buffer_store_b32(v, r, vo, so, OUT_AUX); }

template <int CTRL, int RMASK> DI u32 dpp_mov(u32 x) { return (u32)__builtin_amdgcn_update_dpp(0, (int)x, CTRL, RMASK, 0xf, false); }
DI u32 wave_scan(u32 x) {
  x += dpp_mov<0x111, 0xf>(x);
  x += dpp_mov<0x112, 0xf>(x);
  x += dpp_mov<0x114, 0xf>(x);
  x += dpp_mov<0x118, 0xf>(x);
  x += dpp_mov<0x142, 0xa>(x);
  x += dpp_mov<0x143, 0xc>(x);
  return x;
}
template <int CTRL, int RMASK> DI float dpp_addf(float x) { return x + __uint_as_float(dpp_mov<CTRL, RMASK>(__float_as_uint(x))); }
DI float row_scanf(float x) {
  x = dpp_addf<0x111, 0xf>(x); x = dpp_addf<0x112, 0xf>(x); x = dpp_addf<0x114, 0xf>(x); x = dpp_addf<0x118, 0xf>(x);
  return x;
}
DI float wave_scanf(float x) {
  x = row_scanf(x); x = dpp_addf<0x142, 0xa>(x); x = dpp_addf<0x143, 0xc>(x);
  return x;
}
DI float rdlanef(float x, int l) { return __uint_as_float(__builtin_amdgcn_readlane(__float_as_uint(x), l)); }
DI u32 quadrant(float x, float y, float z, float sx, float sy, float sz) {
  return (x > sx ? 1u : 0u) | (y > sy ? 2u : 0u) | ((CS == 1 && z > sz) ? 4u : 0u);
}

#define STORE_MODE 0
#define OUT_MODE 0
DI void st32o(void* p, u32 v) {
  *(u32*)p = v;
}
DI void st32(void* p, u32 v) {
  *(u32*)p = v;
}

template <int NPR, bool VEC>
DI void filter_role(char* lds, const u16* __restrict__ pos, const int* __restrict__ src, const int* __restrict__ dst,
                    u4* __restrict__ rec, u4* __restrict__ ovf, u32* __restrict__ desc, float* __restrict__ split, int N, int E, int P, u32 magicM) {
  const int tid = threadIdx.x, lane = tid & 63, wv = __builtin_amdgcn_readfirstlane(tid >> 6), p = blockIdx.x;
  u32* hist = (u32*)lds;
  f4* red = (f4*)(lds + NCELL * 4);
  u2* tab = (u2*)(lds + NCELL * 4 + 256);
  TS(0, 0);
  const int e0 = p * CHUNK + tid * 4;
  const int npair = N >> 1;
  const char* posb = (const char*)pos;
  u32 sm0, sm1, sm2;
  { const u32* t = (const u32*)(posb + (u32)(12 * (int)(((unsigned)tid * (unsigned)npair) >> 10))); sm0 = t[0]; sm1 = t[1]; sm2 = t[2]; }
  u32 pw[NPR][3];
#pragma unroll
  for (int r = 0; r < NPR; r++) {
    const u32* t = (const u32*)(posb + (u32)(12 * min(tid + r * 1024, npair - 1)));
    pw[r][0] = t[0]; pw[r][1] = t[1]; pw[r][2] = t[2];
  }
  int sI[4], dI[4];
  if (VEC) {
    const int ec = min(e0, E - 4);
    int4 a = *(const int4*)(src + ec), b = *(const int4*)(dst + ec);
    sI[0] = a.x; sI[1] = a.y; sI[2] = a.z; sI[3] = a.w;
    dI[0] = b.x; dI[1] = b.y; dI[2] = b.z; dI[3] = b.w;
  } else {
#pragma unroll
    for (int u = 0; u < 4; u++) { const int ec = min(e0 + u, E - 1); sI[u] = src[ec]; dI[u] = dst[ec]; }
  }
  if (tid < NCELL) hist[tid] = 0;
  {
    float sx = wave_scanf(lo16(sm0) + hi16(sm1)), sy = wave_scanf(hi16(sm0) + lo16(sm2));
    float sz = CS == 1 ? wave_scanf(lo16(sm1) + hi16(sm2)) : 0.f;
    if (lane == 63) { f4 t = {sx, sy, sz, 0.f}; red[wv] = t; }
  }
#pragma unroll
  for (int r = 0; r < NPR; r++) {
    const int i = tid + r * 1024;
    if (i < npair) {
      u4 t;
      t.x = pw[r][0]; t.y = pw[r][1] & 0xffffu;
      t.z = __builtin_amdgcn_alignbit(pw[r][2], pw[r][1], 16); t.w = pw[r][2] >> 16;
      *(u4*)(tab + 2 * i) = t;
    }
  }
  if ((N & 1) && tid == 0) {
    const u32 n0 = N - 1;
    u2 t = {(u32)pos[3 * n0] | ((u32)pos[3 * n0 + 1] << 16), (u32)pos[3 * n0 + 2]};
    tab[n0] = t;
  }
  TS(0, 1);
  bar_lds();
  TS(0, 2);
  float sx, sy, sz = 0.f;
  {
    f4 t = red[lane & 15];
    sx = rdlanef(row_scanf(t.x), 15) * (1.f / 2048.f); sy = rdlanef(row_scanf(t.y), 15) * (1.f / 2048.f);
    if (CS == 1) sz = rdlanef(row_scanf(t.z), 15) * (1.f / 2048.f);
  }
  if (p == 0 && tid == 0) { split[0] = sx; split[1] = sy; split[2] = sz; }
  u32 rk[4], rxy[4], rz[4];
  u2 ta[4], tb[4];
#pragma unroll
  for (int u = 0; u < 4; u++) { ta[u] = tab[sI[u]]; tb[u] = tab[dI[u]]; }
#pragma unroll
  for (int u = 0; u < 4; u++) {
    const u2 a = ta[u], b = tb[u];
    const float bx = lo16(b.x), by = hi16(b.x);
    const float rx = lo16(a.x) - bx, ry = hi16(a.x) - by, rzz = lo16(a.y) - lo16(b.y);
    const float d2 = rx * rx + ry * ry + rzz * rzz;
    const bool cand = (e0 + u < E) && !(d2 >= 25.25f && d2 <= 3.0e38f);
    rk[u] = 0xffffffffu;
    if (cand) {
      const u32 cell = quadrant(bx, by, lo16(b.y), sx, sy, sz) * JG + __umulhi((u32)dI[u], magicM);
      rk[u] = atomicAdd(&hist[cell], 1u) | (cell << 16);
    }
    rxy[u] = pkbf(rx, ry); rz[u] = pkbf(rzz, 0.f);
  }
  bar_lds();
  TS(0, 3);
  if (wv == 0) {
    constexpr int CPL = NCELL / 64;
    u32 c[CPL], o[CPL], s = 0;
#pragma unroll
    for (int i = 0; i < CPL; i += 4) { u4 t = *(const u4*)(hist + lane * CPL + i); c[i] = t.x; c[i + 1] = t.y; c[i + 2] = t.z; c[i + 3] = t.w; }
#pragma unroll
    for (int i = 0; i < CPL; i++) s += c[i] > SLOTK ? c[i] - SLOTK : 0u;
    u32 ex = wave_scan(s) - s;
#pragma unroll
    for (int i = 0; i < CPL; i++) { o[i] = ex; ex += c[i] > SLOTK ? c[i] - SLOTK : 0u; }
#pragma unroll
    for (int i = 0; i < CPL; i += 4) { u4 t = {o[i], o[i + 1], o[i + 2], o[i + 3]}; *(u4*)(hist + lane * CPL + i) = t; }
#pragma unroll
    for (int i = 0; i < CPL; i++) desc[(lane * CPL + i) * P + p] = c[i] | (o[i] << 16);
  }
#pragma unroll
  for (int u = 0; u < 4; u++) {
    if (rk[u] != 0xffffffffu && (rk[u] & 0xffffu) < SLOTK) {
      u4 t = {(u32)sI[u], (u32)dI[u], rxy[u], rz[u]};
      rec[(size_t)(((rk[u] >> 16) * P + p) * SLOTK + (rk[u] & 0xffffu))] = t;
    }
  }
  bool anyo = false;
#pragma unroll
  for (int u = 0; u < 4; u++) anyo |= rk[u] != 0xffffffffu && (rk[u] & 0xffffu) >= SLOTK;
  bar_lds();
  TS(0, 4);
  if (anyo) {
#pragma unroll
    for (int u = 0; u < 4; u++) {
      if (rk[u] != 0xffffffffu && (rk[u] & 0xffffu) >= SLOTK) {
        const u32 idx = hist[rk[u] >> 16] + (rk[u] & 0xffffu) - SLOTK;
        u4 t = {(u32)sI[u], (u32)dI[u], rxy[u], rz[u]};
        ovf[(size_t)p * CHUNK + idx] = t;
      }
    }
  }
  TS(0, 5);
  TSDRAIN;
  TS(0, 6);
}

constexpr int HP = 272;
constexpr int PP = 784;
#define PHI_AUX 0
DI s8 as_s8(u4 x) { return __builtin_bit_cast(s8, x); }

DI void mlp_role(char* lds, const u16* __restrict__ sv, const u16* __restrict__ w1, const u16* __restrict__ w2,
                 u16* __restrict__ phi, int N, int tile) {
  const int tid = threadIdx.x, lane = tid & 63, wv = __builtin_amdgcn_readfirstlane(tid >> 6);
  const int g = lane >> 4, c = lane & 15;
  const int n0 = tile * (RT * 16);
  TS(0, 0);
  char* sS = lds;
  char* sH = lds + RT * 16 * HP;
  const char* svb = (const char*)sv;
  u4 sreg[2];
#pragma unroll
  for (int r = 0; r < 2; r++) {
    const int i = min(tid + r * 1024, RT * 256 - 1), row = i >> 4, ch = i & 15;
    sreg[r] = *(const u4*)(svb + (u32)(min(n0 + row, N - 1) * 1024 + ch * 16));
  }
  const char* wp = (const char*)((wv < 4) ? (w1 + 32 * wv) : (w2 + 32 * (wv - 4)));
  const int wld = (wv < 4) ? 256 : 768;
  u32 L[32];
  {
    const u32 wo = (u32)(8 * g * wld + 4 * c);
#pragma unroll
    for (int s = 0; s < 4; s++)
#pragma unroll
      for (int j = 0; j < 8; j++) L[s * 8 + j] = *(const u32*)(wp + (size_t)((32 * s + j) * wld) + wo);
  }
#pragma unroll
  for (int r = 0; r < 2; r++) {
    const int i = tid + r * 1024, row = i >> 4, ch = i & 15;
    if (row < RT * 16) *(u4*)(sS + row * HP + ch * 16) = sreg[r];
  }
  TS(0, 1);
  bar_lds();
  TS(0, 2);
  u32 warm;
  {
    const int i = min(tid, RT * 16 * 6 - 1), row = i / 6, ln = i - row * 6;
    warm = *(const u32*)(svb + (u32)(min(n0 + row, N - 1) * 1024 + 256 + ln * 128));
  }
  u4 B[2][4];
  auto make_b = [&]() __attribute__((always_inline)) {
#pragma unroll
    for (int i = 0; i < 32; i += 8)
      asm volatile("" : "+v"(L[i]), "+v"(L[i + 1]), "+v"(L[i + 2]), "+v"(L[i + 3]), "+v"(L[i + 4]), "+v"(L[i + 5]), "+v"(L[i + 6]), "+v"(L[i + 7]));
#pragma unroll
    for (int s = 0; s < 4; s++)
#pragma unroll
      for (int i = 0; i < 4; i++) {
        B[0][s][i] = __builtin_amdgcn_perm(L[s * 8 + 2 * i + 1], L[s * 8 + 2 * i], 0x05040100u);
        B[1][s][i] = __builtin_amdgcn_perm(L[s * 8 + 2 * i + 1], L[s * 8 + 2 * i], 0x07060302u);
      }
  };
  if (wv < 4) {
    make_b();
    f4 a0[RT], a1[RT];
#pragma unroll
    for (int rt = 0; rt < RT; rt++) { f4 z = {0, 0, 0, 0}; a0[rt] = z; a1[rt] = z; }
#pragma unroll
    for (int s = 0; s < 4; s++)
#pragma unroll
      for (int rt = 0; rt < RT; rt++) {
        u4 A = *(const u4*)(sS + (rt * 16 + c) * HP + (32 * s + 8 * g) * 2);
        a0[rt] = __builtin_amdgcn_mfma_f32_16x16x32_bf16(as_s8(A), as_s8(B[0][s]), a0[rt], 0, 0, 0);
        a1[rt] = __builtin_amdgcn_mfma_f32_16x16x32_bf16(as_s8(A), as_s8(B[1][s]), a1[rt], 0, 0, 0);
      }
#pragma unroll
    for (int rt = 0; rt < RT; rt++)
#pragma unroll
      for (int r = 0; r < 4; r++) *(u32*)(sH + (rt * 16 + 4 * g + r) * HP + (32 * wv + 2 * c) * 2) = pkbf(a0[rt][r], a1[rt][r]);
  }
  bar_lds();
#pragma unroll
  for (int k = 0; k < (RT * 16 * 64 + 1023) / 1024; k++) {
    const int i = tid + k * 1024;
    if (i < RT * 16 * 64) {
      u32* hp = (u32*)(sH + (i >> 6) * HP + (i & 63) * 4);
      const u32 xr = *hp;
      const float x0 = lo16(xr), x1 = hi16(xr);
      const float h0 = x0 * __builtin_amdgcn_rcpf(1.f + __builtin_amdgcn_exp2f(x0 * -1.4426950408889634f));
      const float h1 = x1 * __builtin_amdgcn_rcpf(1.f + __builtin_amdgcn_exp2f(x1 * -1.4426950408889634f));
      *hp = pkbf(h0, h1);
    }
  }
  TS(0, 3);
  bar_lds();
  TSW(0, 4, 4);
  if (wv >= 4) {
    make_b();
    const rsrc_t rp = mk_rsrc(phi, (u32)N * 1024u);
    const u32 oo = (u32)((n0 + 4 * g) * 1024 + 64 * (wv - 4) + 4 * c);
#pragma unroll
    for (int rt = 0; rt < RT; rt++) {
      f4 a0 = {0, 0, 0, 0}, a1 = {0, 0, 0, 0};
#pragma unroll
      for (int s = 0; s < 4; s++) {
        u4 A = *(const u4*)(sH + (rt * 16 + c) * HP + (32 * s + 8 * g) * 2);
        a0 = __builtin_amdgcn_mfma_f32_16x16x32_bf16(as_s8(A), as_s8(B[0][s]), a0, 0, 0, 0);
        a1 = __builtin_amdgcn_mfma_f32_16x16x32_bf16(as_s8(A), as_s8(B[1][s]), a1, 0, 0, 0);
      }
#pragma unroll
      for (int r = 0; r < 4; r++) __builtin_amdgcn_raw_buffer_store_b32(pkbf(a0[r], a1[r]), rp, oo + (rt * 16 + r) * 1024, 0, PHI_AUX);
    }
  }
  TSW(0, 5, 4);
  TSDRAIN;
  TSW(0, 6, 4);
  asm volatile("" ::"v"(warm));
}

template <int NPR, bool VEC>
__global__ __launch_bounds__(1024) void k_prep(const u16* __restrict__ sv, const u16* __restrict__ pos, const int* __restrict__ src,
                                               const int* __restrict__ dst, const u16* __restrict__ w1, const u16* __restrict__ w2,
                                               u16* __restrict__ phi, u4* __restrict__ rec, u4* __restrict__ ovf, u32* __restrict__ desc,
                                               float* __restrict__ split, int N, int E, int P, u32 magicM) {
  extern __shared__ __attribute__((aligned(16))) char lds[];
  if ((int)blockIdx.x < P) filter_role<NPR, VEC>(lds, pos, src, dst, rec, ovf, desc, split, N, E, P, magicM);
  else mlp_role(lds, sv, w1, w2, phi, N, blockIdx.x - P);
}

#define K2W 8
#define K2OCC 4
constexpr int NW = K2W;
constexpr int NT = NW * 64;
constexpr int CH = NT;
constexpr int SLOTS = CH + (CS == 2 ? MMAX : 0) + 8;
constexpr int LPC = 64 / CS;
constexpr int L_PA = 0;
constexpr int L_PB = L_PA + SLOTS * 16;
constexpr int L_RAW = L_PB;
constexpr int L_CNT = L_PB + SLOTS * 16;
constexpr int L_START = L_CNT + MMAX * 4;
constexpr int L_MISC = L_START + MMAX * 4;
constexpr int L_HT = L_MISC + 512;
constexpr int L_W = L_HT + 2 * NW * 1024 * (2 / CS);
constexpr int L_TOTAL = L_W + 6 * LPC * 8;

struct TrueT { static constexpr bool value = true; };
struct FalseT { static constexpr bool value = false; };
DI f4 mfma444(u2 a, u2 b, f4 c) {
  return __builtin_amdgcn_mfma_f32_4x4x4bf16_1k(__builtin_bit_cast(s4, a), __builtin_bit_cast(s4, b), c, 0, 0, 0);
}

DI void agg_range(char* lds, rsrc_t rphi, rsrc_t rsv, rsrc_t rres, rsrc_t rout, u32 lofs, u32 nb, int lane, int wv, int itb, int ite) {
  const int half = CS == 2 ? lane >> 5 : 0, lp = lane & (LPC - 1);
  u2 wt[3][2];
#pragma unroll
  for (int g = 0; g < 3; g++)
#pragma unroll
    for (int t = 0; t < 2; t++) wt[g][t] = *(const u2*)(lds + L_W + ((g * 2 + t) * LPC + lp) * 8);
  u2 ident;
  ident.x = (lane & 3) == 0 ? 0x00003f80u : ((lane & 3) == 1 ? 0x3f800000u : 0u);
  ident.y = (lane & 3) == 2 ? 0x00003f80u : ((lane & 3) == 3 ? 0x3f800000u : 0u);
  const f4 zero = {0, 0, 0, 0};
  f2 acc[4];
#pragma unroll
  for (int r = 0; r < 4; r++) { f2 z = {0.f, 0.f}; acc[r] = z; }
  const u32 olane = (CS == 2 ? half * 512u : 0u) + lofs;
  const char* pbl = lds + L_PB + half * 16 + itb * (CS * 16);
  const char* pal = lds + L_PA + half * 16 + (itb + (lane & 3)) * (CS * 16);
  constexpr int NO = 2 / CS;
  char* hl = lds + L_HT + (wv * 64 + lane) * (16 * NO);
  bool pend = itb > 0 && itb < ite && (__builtin_amdgcn_readfirstlane(*(const u32*)(lds + L_PB + (itb - 1) * (CS * 16) + 12)) & 1u) == 0;
  u32 headso = 0xffffffffu, lastw = 1u;
  for (int it = itb; it < ite; it += 4, pbl += 4 * CS * 16, pal += 4 * CS * 16) {
    u32 w[4];
#pragma unroll
    for (int u = 0; u < 4; u++) w[u] = *(const u32*)(pbl + u * CS * 16 + 12);
    const u4 am = *(const u4*)pal;
    u2 b[4][3];
    u32 sw[4], q[4][4 / CS];
#pragma unroll
    for (int u = 0; u < 4; u++) {
      sw[u] = __builtin_amdgcn_readfirstlane(w[u]);
      if (u == 0 || it + u < ite) {
        const u32 vo = (w[u] & 0xfffffc00u) | lofs;
        b[u][0].x = bld(rphi, vo, 0);
        b[u][0].y = bld(rphi, vo + 256, 0);
        b[u][1].x = bld(rphi, vo + 512, 0);
        b[u][1].y = bld(rsv, vo + 256, 0);
        b[u][2].x = bld(rsv, vo + 512, 0);
        b[u][2].y = bld(rsv, vo + 768, 0);
        if (sw[u] & 1u) {
          const u32 so = (nb + ((sw[u] >> 1) & 0x1ffu)) << 10;
#pragma unroll
          for (int r = 0; r < 4 / CS; r++) q[u][r] = bld(rres, olane + r * 256, so);
        }
      }
    }
    asm volatile("" ::: "memory");
    f4 wf[3][2];
#pragma unroll
    for (int g = 0; g < 3; g++)
#pragma unroll
      for (int t = 0; t < 2; t++) {
        u2 ah = {am.x, am.y}, al = {am.z, am.w};
        wf[g][t] = mfma444(al, wt[g][t], mfma444(ah, wt[g][t], zero));
      }
#pragma unroll
    for (int u = 0; u < 4; u++) {
      if (it + u < ite) {
        const u4 rb = *(const u4*)(pbl + u * CS * 16);
        const f4 U0 = mfma444(ident, b[u][0], zero), U1 = mfma444(ident, b[u][1], zero), U2 = mfma444(ident, b[u][2], zero);
        const float r0 = __uint_as_float(rb.x), r1 = __uint_as_float(rb.y), r2 = __uint_as_float(rb.z);
        acc[0].x = __builtin_fmaf(U0[0], wf[0][0][u], acc[0].x);
        acc[0].y = __builtin_fmaf(U0[1], wf[0][1][u], acc[0].y);
        const float mv0 = U0[2] * wf[1][0][u], mv1 = U0[3] * wf[1][1][u];
        const float mr0 = U1[0] * wf[2][0][u], mr1 = U1[1] * wf[2][1][u];
        acc[1].x = __builtin_fmaf(mv0, U1[2], acc[1].x); acc[1].y = __builtin_fmaf(mv1, U1[3], acc[1].y);
        acc[2].x = __builtin_fmaf(mv0, U2[0], acc[2].x); acc[2].y = __builtin_fmaf(mv1, U2[1], acc[2].y);
        acc[3].x = __builtin_fmaf(mv0, U2[2], acc[3].x); acc[3].y = __builtin_fmaf(mv1, U2[3], acc[3].y);
        acc[1].x = __builtin_fmaf(mr0, r0, acc[1].x); acc[1].y = __builtin_fmaf(mr1, r0, acc[1].y);
        acc[2].x = __builtin_fmaf(mr0, r1, acc[2].x); acc[2].y = __builtin_fmaf(mr1, r1, acc[2].y);
        acc[3].x = __builtin_fmaf(mr0, r2, acc[3].x); acc[3].y = __builtin_fmaf(mr1, r2, acc[3].y);
        lastw = sw[u];
        if (sw[u] & 1u) {
          const u32 so = (nb + ((sw[u] >> 1) & 0x1ffu)) << 10;
          f4 o[NO];
          if (CS == 2) {
#pragma unroll
            for (int r = 0; r < 2; r++) {
              auto x = __builtin_amdgcn_permlane32_swap(__float_as_uint(acc[r].x), __float_as_uint(acc[r + 2].x), false, false);
              auto y = __builtin_amdgcn_permlane32_swap(__float_as_uint(acc[r].y), __float_as_uint(acc[r + 2].y), false, false);
              o[0][2 * r] = __uint_as_float(x[0]) + __uint_as_float(x[1]);
              o[0][2 * r + 1] = __uint_as_float(y[0]) + __uint_as_float(y[1]);
            }
          } else {
#pragma unroll
            for (int r = 0; r < NO; r++) { f4 t = {acc[2 * r].x, acc[2 * r].y, acc[2 * r + 1].x, acc[2 * r + 1].y}; o[r] = t; }
          }
#pragma unroll
          for (int r = 0; r < NO; r++) { u2 qq = {q[u][(2 * r) % (4 / CS)], q[u][(2 * r + 1) % (4 / CS)]}; o[r] = mfma444(ident, qq, o[r]); }
          if (pend) {
#pragma unroll
            for (int r = 0; r < NO; r++) *(f4*)(hl + r * 16) = o[r];
            headso = so; pend = false;
          } else {
#pragma unroll
            for (int r = 0; r < NO; r++) {
              bst(pkbf(o[r][0], o[r][1]), rout, olane + r * 512, so);
              bst(pkbf(o[r][2], o[r][3]), rout, olane + r * 512 + 256, so);
            }
          }
#pragma unroll
          for (int r = 0; r < 4; r++) { f2 z = {0.f, 0.f}; acc[r] = z; }
        }
      }
    }
  }
  const bool tail = (lastw & 1u) == 0;
  if (tail) {
    f4 o[NO];
    if (CS == 2) {
#pragma unroll
      for (int r = 0; r < 2; r++) {
        auto x = __builtin_amdgcn_permlane32_swap(__float_as_uint(acc[r].x), __float_as_uint(acc[r + 2].x), false, false);
        auto y = __builtin_amdgcn_permlane32_swap(__float_as_uint(acc[r].y), __float_as_uint(acc[r + 2].y), false, false);
        o[0][2 * r] = __uint_as_float(x[0]) + __uint_as_float(x[1]);
        o[0][2 * r + 1] = __uint_as_float(y[0]) + __uint_as_float(y[1]);
      }
    } else {
#pragma unroll
      for (int r = 0; r < NO; r++) { f4 t = {acc[2 * r].x, acc[2 * r].y, acc[2 * r + 1].x, acc[2 * r + 1].y}; o[r] = t; }
    }
#pragma unroll
    for (int r = 0; r < NO; r++) *(f4*)(hl + NW * 64 * 16 * NO + r * 16) = o[r];
  }
  if (lane == 0) *(u32*)(lds + L_MISC + (32 + wv) * 4) = (tail ? 1u : 0u) | ((pend || itb >= ite) ? 2u : 0u);
  bar_lds();
  if (headso != 0xffffffffu) {
    f4 o[NO];
#pragma unroll
    for (int r = 0; r < NO; r++) o[r] = *(const f4*)(hl + r * 16);
    for (int k = wv - 1; k >= 0; k--) {
      const u32 st = __builtin_amdgcn_readfirstlane(*(const u32*)(lds + L_MISC + (32 + k) * 4));
      if (st & 1u) {
#pragma unroll
        for (int r = 0; r < NO; r++) o[r] += *(const f4*)(lds + L_HT + ((NW + k) * 64 + lane) * (16 * NO) + r * 16);
      }
      if (!(st & 2u)) break;
    }
#pragma unroll
    for (int r = 0; r < NO; r++) {
      bst(pkbf(o[r][0], o[r][1]), rout, olane + r * 512, headso);
      bst(pkbf(o[r][2], o[r][3]), rout, olane + r * 512 + 256, headso);
    }
  }
}

NOPK __global__ __launch_bounds__(NT, K2OCC) void k_agg(const u16* __restrict__ sv, const u16* __restrict__ pos, const u16* __restrict__ freqs,
                                                        const u16* __restrict__ wr, const u16* __restrict__ phi, const u4* __restrict__ rec,
                                                        const u4* __restrict__ ovf, const u32* __restrict__ desc,
                                                        const float* __restrict__ split, u16* out, int N, int P, int M) {
  __shared__ __attribute__((aligned(16))) char lds[L_TOTAL];
  const int tid = threadIdx.x, lane = tid & 63, wv = __builtin_amdgcn_readfirstlane(tid >> 6);
  const int label = blockIdx.x & 7, q = CS == 2 ? label >> 1 : label, h = CS == 2 ? label & 1 : 0, j = blockIdx.x >> 3;
  const int cell = q * JG + j, nb = j * M;
  const int half = CS == 2 ? lane >> 5 : 0, lp = lane & (LPC - 1);
  u32* cnt = (u32*)(lds + L_CNT);
  u32* start = (u32*)(lds + L_START);
  u32* misc = (u32*)(lds + L_MISC);
  u4* raw = (u4*)(lds + L_RAW);
  u4* pa = (u4*)(lds + L_PA);
  u4* pb = (u4*)(lds + L_PB);
  const char* svb = (const char*)sv;
  const int nmw = (M + 63) >> 6;
  constexpr int RND = (PMAX * SLOTK + NT - 1) / NT;

  TS(1, 0);
  u32 dsc[RND];
  u4 pre[RND];
#pragma unroll
  for (int i = 0; i < RND; i++) {
    const u32 sl = min(tid + i * NT, P * SLOTK - 1);
    dsc[i] = desc[cell * P + (sl / SLOTK)];
    pre[i] = rec[(u32)cell * (u32)(P * SLOTK) + sl];
  }
  const u32 nc = min(nb + tid, N - 1);
  const u16* pp = (const u16*)((const char*)pos + nc * 6u);
  const u32 px = pp[0], py = pp[1], pz = CS == 1 ? pp[2] : 0u;
  constexpr int NWE = 6 * LPC;
  static_assert(NWE <= NT, "W staging");
  u32 wk[4];
  {
    const int we = min(tid, NWE - 1), gt = we / LPC, l = we % LPC;
    const u16* wp = wr + (gt >> 1) * 128 + h * 64 + 2 * l + (gt & 1);
#pragma unroll
    for (int k = 0; k < 4; k++) wk[k] = wp[k * 384];
  }
  const float sx = split[0], sy = split[1], sz = CS == 1 ? split[2] : 0.f;
  const u32 fq01 = *(const u32*)freqs, fq23 = *(const u32*)(freqs + 2);
  for (int i = tid; i < MMAX; i += NT) cnt[i] = 0;
  if (tid < 2) misc[tid] = 0;
  bar_lds();

  u32 pendm = 0;
  {
    unsigned long long vm[RND], vm2[RND];
    u4 pre2[RND];
    bool ovfl = false;
#pragma unroll
    for (int i = 0; i < RND; i++) {
      const int sl = tid + i * NT;
      const u32 c = sl < P * SLOTK ? dsc[i] & 0xffffu : 0u, k = sl % SLOTK;
      const bool v2 = k + SLOTK < c;
      vm[i] = ballot64(k < c);
      vm2[i] = ballot64(v2);
      if (vm2[i]) pre2[i] = ovf[v2 ? (u32)(sl / SLOTK) * CHUNK + (dsc[i] >> 16) + k : 0u];
      ovfl |= c > 2 * SLOTK;
    }
    u32 tot = 0;
#pragma unroll
    for (int i = 0; i < RND; i++) tot += __popcll(vm[i]) + __popcll(vm2[i]);
    u32 base = 0;
    if (lane == 0) base = atomicAdd(&misc[0], tot);
    if (ballot64(ovfl) != 0 && lane == 0) misc[1] = 1;
    base = __builtin_amdgcn_readfirstlane(base);
#pragma unroll
    for (int i = 0; i < RND; i++) {
      const u32 pos = base + __builtin_amdgcn_mbcnt_hi((u32)(vm[i] >> 32), __builtin_amdgcn_mbcnt_lo((u32)vm[i], 0));
      base += __popcll(vm[i]);
      if (vm[i] >> lane & 1) { if (pos < CH) raw[pos] = pre[i]; else pendm |= 1u << i; }
    }
#pragma unroll
    for (int i = 0; i < RND; i++) {
      if (vm2[i]) {
        const u32 pos = base + __builtin_amdgcn_mbcnt_hi((u32)(vm2[i] >> 32), __builtin_amdgcn_mbcnt_lo((u32)vm2[i], 0));
        base += __popcll(vm2[i]);
        if (vm2[i] >> lane & 1) { if (pos < CH) raw[pos] = pre2[i]; else pendm |= 1u << (RND + i); }
      }
    }
  }
  const bool member = tid < M && nb + tid < N && quadrant(lo16(px), lo16(py), lo16(pz), sx, sy, sz) == (u32)q;
  u32 touch[8 / CS];
#pragma unroll
  for (int i = 0; i < 8 / CS; i++) touch[i] = *(const u32*)(svb + (member ? nc * 1024u + (CS == 2 ? i * 256u + h * 128u : i * 128u) : 0u));
  if (tid < NWE) { u2 t = {wk[0] | (wk[1] << 16), wk[2] | (wk[3] << 16)}; *(u2*)(lds + L_W + tid * 8) = t; }
  const u32 lofs = h * 128 + lp * 4;
  const rsrc_t rphi = mk_rsrc(phi, (u32)N * 1024u), rsv = mk_rsrc(sv, (u32)N * 1024u), rout = mk_rsrc(out, (u32)N * 1024u);
  TS(1, 1);
  bar_lds();
  TS(1, 2);
  u32 nslot = __builtin_amdgcn_readfirstlane(misc[0]);
  const bool anyovf = __builtin_amdgcn_readfirstlane(misc[1]) != 0;
  u32 total = nslot;
  auto stage_ovf = [&](u32 c0) __attribute__((always_inline)) -> u32 {
    u32 pos = nslot;
    for (int p0 = 0; p0 < P; p0 += 64) {
      const u32 d = p0 + lane < P ? desc[cell * P + p0 + lane] : 0u;
      const u32 c = d & 0xffffu, n = c > 2 * SLOTK ? c - 2 * SLOTK : 0u;
      const u32 inc = wave_scan(n);
      if (wv == 0) {
        const u32 my = pos + inc - n;
        for (u32 k = 0; k < n; k++)
          if (my + k >= c0 && my + k < c0 + CH) raw[my + k - c0] = ovf[(size_t)(p0 + lane) * CHUNK + (d >> 16) + SLOTK + k];
      }
      pos += __builtin_amdgcn_readlane(inc, 63);
    }
    return pos;
  };
  if (anyovf) { total = stage_ovf(0u); bar_lds(); }

  auto process = [&](auto first_tag, u32 nrec) __attribute__((always_inline)) {
    constexpr bool FIRST = decltype(first_tag)::value;
    bool act = false; u32 loc = 0, rnk = 0;
    u4 ca = {0, 0, 0, 0}, cb = {0, 0, 0, 0};
    if ((u32)(wv * 64) < nrec) {
      const float frq[4] = {lo16(fq01), hi16(fq01), lo16(fq23), hi16(fq23)};
      const u4 t = raw[tid];
      loc = t.y - nb;
      const float rx = lo16(t.z), ry = hi16(t.z), rz = lo16(t.w);
      const u32 s01 = pkbf(rx * rx, ry * ry);
      float ss = lo16(s01) + hi16(s01) + rbf(rz * rz);
      ss = rbf(ss); ss = rbf(ss + 1e-8f);
      const float dd = rbf(__builtin_amdgcn_sqrtf(ss));
      act = (u32)tid < nrec && !(dd >= 5.0f && dd <= 3.0e38f);
      if (act) rnk = atomicAdd(&cnt[loc], 1u);
      const float id = __builtin_amdgcn_rcpf(dd);
      const u32 rh01 = pkbf(rx * id, ry * id);
      cb.x = rh01 << 16; cb.y = rh01 & 0xffff0000u; cb.z = pkbf(0.f, rz * id) & 0xffff0000u; cb.w = t.x;
      float fc = rbf(3.14159265358979f * dd);
      fc = rbf(fc * 0.2f);
      fc = rbf(__builtin_amdgcn_cosf(__builtin_amdgcn_fractf(fc * 0.15915494309189535f)));
      fc = 0.5f * rbf(fc + 1.0f);
      if (!(dd < 5.0f)) fc = 0.0f * dd;
      float a[4];
#pragma unroll
      for (int k = 0; k < 4; k += 2) {
        const u32 x = pkbf(dd * frq[k], dd * frq[k + 1]);
        const u32 y = pkbf(__builtin_amdgcn_sinf(__builtin_amdgcn_fractf(lo16(x) * 0.15915494309189535f)),
                           __builtin_amdgcn_sinf(__builtin_amdgcn_fractf(hi16(x) * 0.15915494309189535f)));
        const u32 z = pkbf(lo16(y) * id, hi16(y) * id);
        a[k] = lo16(z) * fc; a[k + 1] = hi16(z) * fc;
      }
      ca.x = pkbf(a[0], a[1]); ca.y = pkbf(a[2], a[3]);
      ca.z = pkbf(a[0] - lo16(ca.x), a[1] - hi16(ca.x)); ca.w = pkbf(a[2] - lo16(ca.y), a[3] - hi16(ca.y));
    }
    bar_lds();
    TS(1, 4);
    int T;
    {
      int s4[4];
      const u4 c4 = *(const u4*)(cnt + lane * 4);
      u32 it[4] = {c4.x, c4.y, c4.z, c4.w}, sum = 0;
#pragma unroll
      for (int i = 0; i < 4; i++) { if (CS == 2) it[i] = (it[i] + 1) >> 1; sum += it[i]; }
      const u32 inc = wave_scan(sum);
      T = (int)__builtin_amdgcn_readlane(inc, 63);
      u32 ex = inc - sum;
#pragma unroll
      for (int i = 0; i < 4; i++) { s4[i] = (int)ex; ex += it[i]; }
      u4 sv4 = {(u32)s4[0], (u32)s4[1], (u32)s4[2], (u32)s4[3]};
      *(u4*)(start + lane * 4) = sv4;
    }
    if (act) {
      const u32 st = start[loc], sl = CS * st + rnk;
      const u32 cc = cnt[loc];
      const u32 last = CS == 2 ? ((rnk | 1u) + 1 >= cc) : (rnk + 1 == cc);
      cb.w = (cb.w << 10) | (loc << 1) | last;
      pa[sl] = ca; pb[sl] = cb;
      if (CS == 2 && rnk + 1 == cc && (cc & 1)) { u4 z = {0, 0, 0, 0}; pa[sl + 1] = z; u4 t = {0, 0, 0, cb.w}; pb[sl + 1] = t; }
    }
    bar_lds();
    TS(1, 5);
    const int itb = (wv * T) / NW, ite = ((wv + 1) * T) / NW;
    if (FIRST) {
#pragma unroll
      for (int i = 0; i < 8 / CS; i++) asm volatile("" ::"v"(touch[i]));
    }
    TS(1, 6);
    agg_range(lds, rphi, rsv, FIRST ? rsv : rout, rout, lofs, nb, lane, wv, itb, ite);
    TS(1, 7);
  };
  TS(1, 3);
  process(TrueT(), min(total, (u32)CH));
  if (wv < nmw) {
    unsigned long long em = ballot64(member && cnt[min(tid, MMAX - 1)] == 0);
    while (em) {
      const int l = __builtin_ctzll(em); em &= em - 1;
      const size_t go = (size_t)(nb + wv * 64 + l) * 1024 + (CS == 2 ? half * 512u : 0u) + lofs;
#pragma unroll
      for (int r = 0; r < 4 / CS; r++) *(u32*)((char*)out + go + r * 256) = *(const u32*)(svb + go + r * 256);
    }
  }
  if (total > CH) {
    u32 left = nslot > CH ? nslot - CH : 0u;
    while (left) {
      bar_all();
      for (int i = tid; i < MMAX; i += NT) cnt[i] = 0;
      if (tid == 0) misc[0] = 0;
      bar_lds();
#pragma unroll
      for (int i = 0; i < 2 * RND; i++) {
        const unsigned long long m = ballot64(pendm >> i & 1);
        if (m) {
          u32 base = 0;
          if (lane == 0) base = atomicAdd(&misc[0], (u32)__popcll(m));
          base = __builtin_amdgcn_readfirstlane(base);
          const u32 pos = base + __builtin_amdgcn_mbcnt_hi((u32)(m >> 32), __builtin_amdgcn_mbcnt_lo((u32)m, 0));
          if ((pendm >> i & 1) && pos < CH) {
            const int sl = tid + (i % RND) * NT;
            if (i < RND) raw[pos] = rec[(u32)cell * (u32)(P * SLOTK) + sl];
            else raw[pos] = ovf[(u32)(sl / SLOTK) * CHUNK + (desc[cell * P + sl / SLOTK] >> 16) + sl % SLOTK];
            pendm &= ~(1u << i);
          }
        }
      }
      bar_lds();
      const u32 n = min(left, (u32)CH);
      process(FalseT(), n);
      left -= n;
    }
    if (anyovf) {
      const u32 lo = max(nslot, (u32)CH);
      for (u32 c0 = lo; c0 < total; c0 += CH) {
        bar_all();
        for (int i = tid; i < MMAX; i += NT) cnt[i] = 0;
        stage_ovf(c0);
        bar_lds();
        process(FalseT(), min(total - c0, (u32)CH));
      }
    }
  }
  TS(1, 8);
  TSDRAIN;
  TS(1, 9);
}

__global__ void fb_mlp(const u16* sv, const u16* w1, const u16* w2, u16* phi) {
  __shared__ float s[128], hh[128];
  int n = blockIdx.x, t = threadIdx.x;
  s[t] = lo16(sv[(size_t)n * 512 + t]);
  __syncthreads();
  float acc = 0.f;
  for (int k = 0; k < 128; k++) acc += s[k] * lo16(w1[k * 128 + t]);
  acc = rbf(acc);
  hh[t] = rbf(acc / (1.f + expf(-acc)));
  __syncthreads();
  for (int g = 0; g < 3; g++) {
    float a = 0.f;
    for (int k = 0; k < 128; k++) a += hh[k] * lo16(w2[k * 384 + g * 128 + t]);
    phi[(size_t)n * 384 + g * 128 + t] = (u16)pkbf(a, 0.f);
  }
}
__global__ void fb_edge(const u16* sv, const u16* pos, const int* src, const int* dst, const u16* freqs, const u16* wr,
                        const u16* phi, float* acc) {
  int e = blockIdx.x, t = threadIdx.x;
  size_t s = src[e], d = dst[e];
  float r[3], ss = 0.f;
  for (int x = 0; x < 3; x++) { r[x] = rbf(lo16(pos[s * 3 + x]) - lo16(pos[d * 3 + x])); ss += rbf(r[x] * r[x]); }
  ss = rbf(ss); ss = rbf(ss + 1e-8f);
  float dd = rbf(sqrtf(ss));
  if (dd >= 5.0f && dd <= 3.0e38f) return;
  float fc = rbf(3.14159265358979f * dd); fc = rbf(fc / 5.0f); fc = rbf(cosf(fc)); fc = rbf(fc + 1.0f); fc = rbf(0.5f * fc);
  if (!(dd < 5.0f)) fc = 0.0f * dd;
  float wf[3] = {0.f, 0.f, 0.f};
  for (int k = 0; k < 4; k++) {
    float a = rbf(rbf(sinf(rbf(dd * lo16(freqs[k])))) / dd) * fc;
    for (int g = 0; g < 3; g++) wf[g] += a * lo16(wr[k * 384 + g * 128 + t]);
  }
  atomicAdd(&acc[d * 512 + t], lo16(phi[s * 384 + t]) * wf[0]);
  float mv = lo16(phi[s * 384 + 128 + t]) * wf[1], mr = lo16(phi[s * 384 + 256 + t]) * wf[2];
  for (int x = 0; x < 3; x++) atomicAdd(&acc[d * 512 + 128 + x * 128 + t], mv * lo16(sv[s * 512 + 128 + x * 128 + t]) + mr * rbf(r[x] / dd));
}
__global__ void fb_fin(const u16* sv, const float* acc, u16* out, size_t n) {
  size_t i = blockIdx.x * (size_t)256 + threadIdx.x;
  if (i < n) out[i] = (u16)pkbf(lo16(sv[i]) + acc[i], 0.f);
}

extern "C" void kernel_launch(void* const* d_in, const int* in_sizes, int n_in, void* d_out, int out_size, void* d_ws,
                              size_t ws_size, hipStream_t stream) {
  const u16* sv = (const u16*)d_in[0];
  const u16* pos = (const u16*)d_in[1];
  const int* src = (const int*)d_in[2];
  const int* dst = (const int*)d_in[3];
  const u16* freqs = (const u16*)d_in[4];
  const u16* w1 = (const u16*)d_in[5];
  const u16* w2 = (const u16*)d_in[6];
  const u16* wr = (const u16*)d_in[7];
  u16* out = (u16*)d_out;
  const int N = in_sizes[0] / 512, E = in_sizes[2];
  const int P = (E + CHUNK - 1) / CHUNK;
  const int M = (N + JG - 1) / JG;
  char* ws = (char*)d_ws;
  u16* phi = (u16*)ws;
  size_t off = ((size_t)N * 1024 + 255) & ~(size_t)255;
  if (N >= 2 && E >= 4 && M <= MMAX && M <= NT && N <= 20000 && P <= PMAX) {
    u4* rec = (u4*)(ws + off); off += (size_t)NCELL * P * SLOTK * 16;
    u4* ovf = (u4*)(ws + off); off += (size_t)P * CHUNK * 16;
    u32* desc = (u32*)(ws + off); off += (((size_t)P * NCELL * 4) + 255) & ~(size_t)255;
    float* split = (float*)(ws + off);
    const u32 magicM = (u32)(0x100000000ull / (u32)M) + 1u;
    const int nmlp = (N + RT * 16 - 1) / (RT * 16);
    size_t lds1 = (size_t)((N + 1) & ~1) * 8 + NCELL * 4 + 256;
    const size_t ldsm = 2 * RT * 16 * HP + RT * 16 * PP;
    if (lds1 < ldsm) lds1 = ldsm;
#define LAUNCH_PREP(NPR_, VEC_) do { \
      (void)hipFuncSetAttribute((const void*)k_prep<NPR_, VEC_>, hipFuncAttributeMaxDynamicSharedMemorySize, 160 * 1024); \
      k_prep<NPR_, VEC_><<<P + nmlp, 1024, lds1, stream>>>(sv, pos, src, dst, w1, w2, phi, rec, ovf, desc, split, N, E, P, magicM); } while (0)
    if (N <= 7 * 2048 && (E & 3) == 0) LAUNCH_PREP(7, true);
    else if ((E & 3) == 0) LAUNCH_PREP(10, true);
    else LAUNCH_PREP(10, false);
    k_agg<<<8 * JG, NT, 0, stream>>>(sv, pos, freqs, wr, phi, rec, ovf, desc, split, out, N, P, M);
  } else {
    float* acc = (float*)(ws + off);
    (void)hipMemsetAsync(acc, 0, (size_t)N * 512 * 4, stream);
    fb_mlp<<<N, 128, 0, stream>>>(sv, w1, w2, phi);
    if (E > 0) fb_edge<<<E, 128, 0, stream>>>(sv, pos, src, dst, freqs, wr, phi, acc);
    fb_fin<<<(unsigned)(((size_t)N * 512 + 255) / 256), 256, 0, stream>>>(sv, acc, out, (size_t)N * 512);
  }
}
```

```cpp
#include <hip/hip_runtime.h>
#include <stdint.h>

typedef unsigned short u16;
typedef unsigned int u32;
typedef float f2 __attribute__((ext_vector_type(2)));
typedef float f4 __attribute__((ext_vector_type(4)));
typedef u32 u2 __attribute__((ext_vector_type(2)));
typedef u32 u4 __attribute__((ext_vector_type(4)));
typedef short s4 __attribute__((ext_vector_type(4)));
typedef short s8 __attribute__((ext_vector_type(8)));
typedef __bf16 bf2 __attribute__((ext_vector_type(2)));

#define DI __device__ __forceinline__
#if defined(__HIP_DEVICE_COMPILE__)
#define NOPK __attribute__((target("no-packed-fp32-ops")))
#else
#define NOPK
#endif
#define TS(k, i)
#define TSW(k, i, w)
#define TSDRAIN

#define CSPLIT 2
#define JGROUPS 64
constexpr int CS = CSPLIT;
constexpr int NQ = 8 / CS;
constexpr int JG = JGROUPS;
constexpr int NCELL = NQ * JG;
constexpr int CHUNK = 4096;
#define RTILES 5
constexpr int RT = RTILES;
constexpr int MPL = 4;
constexpr int MMAX = MPL * 64;
constexpr int PMAX = 128;
constexpr int SLOTK = 8;

DI float lo16(u32 x) { return __uint_as_float(x << 16); }
DI float hi16(u32 x) { return __uint_as_float(x & 0xffff0000u); }
DI u32 pkbf(float a, float b) { f2 f = {a, b}; bf2 v = __builtin_convertvector(f, bf2); return __builtin_bit_cast(u32, v); }
DI float rbf(float a) { return lo16(pkbf(a, 0.f)); }

DI void bar_lds() {
  asm volatile("s_waitcnt lgkmcnt(0)" ::: "memory");
  __builtin_amdgcn_s_barrier();
  asm volatile("" ::: "memory");
}
DI void bar_all() {
  asm volatile("s_waitcnt vmcnt(0) lgkmcnt(0)" ::: "memory");
  __builtin_amdgcn_s_barrier();
  asm volatile("" ::: "memory");
}
DI unsigned long long ballot64(bool p) { return __builtin_amdgcn_ballot_w64(p); }
typedef __amdgpu_buffer_rsrc_t rsrc_t;
DI rsrc_t mk_rsrc(const void* p, u32 bytes) { return __builtin_amdgcn_make_buffer_rsrc((void*)p, 0, bytes, 0x00020000); }
DI u32 bld(rsrc_t r, u32 vo, u32 so) { return __builtin_amdgcn_raw_buffer_load_b32(r, vo, so, 0); }
#define OUT_AUX 2
#define NOWARM
DI void bst(u32 v, rsrc_t r, u32 vo, u32 so) { __builtin_amdgcn_raw_buffer_store_b32(v, r, vo, so, OUT_AUX); }

template <int CTRL, int RMASK> DI u32 dpp_mov(u32 x) { return (u32)__builtin_amdgcn_update_dpp(0, (int)x, CTRL, RMASK, 0xf, false); }
DI u32 wave_scan(u32 x) {
  x += dpp_mov<0x111, 0xf>(x);
  x += dpp_mov<0x112, 0xf>(x);
  x += dpp_mov<0x114, 0xf>(x);
  x += dpp_mov<0x118, 0xf>(x);
  x += dpp_mov<0x142, 0xa>(x);
  x += dpp_mov<0x143, 0xc>(x);
  return x;
}
template <int CTRL, int RMASK> DI float dpp_addf(float x) { return x + __uint_as_float(dpp_mov<CTRL, RMASK>(__float_as_uint(x))); }
DI float row_scanf(float x) {
  x = dpp_addf<0x111, 0xf>(x); x = dpp_addf<0x112, 0xf>(x); x = dpp_addf<0x114, 0xf>(x); x = dpp_addf<0x118, 0xf>(x);
  return x;
}
DI float wave_scanf(float x) {
  x = row_scanf(x); x = dpp_addf<0x142, 0xa>(x); x = dpp_addf<0x143, 0xc>(x);
  return x;
}
DI float rdlanef(float x, int l) { return __uint_as_float(__builtin_amdgcn_readlane(__float_as_uint(x), l)); }
DI u32 quadrant(float x, float y, float z, float sx, float sy, float sz) {
  return (x > sx ? 1u : 0u) | (y > sy ? 2u : 0u) | ((CS == 1 && z > sz) ? 4u : 0u);
}

#define STORE_MODE 0
#define OUT_MODE 0
DI void st32o(void* p, u32 v) {
  *(u32*)p = v;
}
DI void st32(void* p, u32 v) {
  *(u32*)p = v;
}

template <int NPR, bool VEC>
DI void filter_role(char* lds, const u16* __restrict__ pos, const int* __restrict__ src, const int* __restrict__ dst,
                    u4* __restrict__ rec, u4* __restrict__ ovf, u32* __restrict__ desc, float* __restrict__ split, int N, int E, int P, u32 magicM) {
  const int tid = threadIdx.x, lane = tid & 63, wv = __builtin_amdgcn_readfirstlane(tid >> 6), p = blockIdx.x;
  u32* hist = (u32*)lds;
  f4* red = (f4*)(lds + NCELL * 4);
  u2* tab = (u2*)(lds + NCELL * 4 + 256);
  TS(0, 0);
  const int e0 = p * CHUNK + tid * 4;
  const int npair = N >> 1;
  const char* posb = (const char*)pos;
  u32 sm0, sm1, sm2;
  { const u32* t = (const u32*)(posb + (u32)(12 * (int)(((unsigned)tid * (unsigned)npair) >> 10))); sm0 = t[0]; sm1 = t[1]; sm2 = t[2]; }
  u32 pw[NPR][3];
#pragma unroll
  for (int r = 0; r < NPR; r++) {
    const u32* t = (const u32*)(posb + (u32)(12 * min(tid + r * 1024, npair - 1)));
    pw[r][0] = t[0]; pw[r][1] = t[1]; pw[r][2] = t[2];
  }
  int sI[4], dI[4];
  if (VEC) {
    const int ec = min(e0, E - 4);
    int4 a = *(const int4*)(src + ec), b = *(const int4*)(dst + ec);
    sI[0] = a.x; sI[1] = a.y; sI[2] = a.z; sI[3] = a.w;
    dI[0] = b.x; dI[1] = b.y; dI[2] = b.z; dI[3] = b.w;
  } else {
#pragma unroll
    for (int u = 0; u < 4; u++) { const int ec = min(e0 + u, E - 1); sI[u] = src[ec]; dI[u] = dst[ec]; }
  }
  if (tid < NCELL) hist[tid] = 0;
  {
    float sx = wave_scanf(lo16(sm0) + hi16(sm1)), sy = wave_scanf(hi16(sm0) + lo16(sm2));
    float sz = CS == 1 ? wave_scanf(lo16(sm1) + hi16(sm2)) : 0.f;
    if (lane == 63) { f4 t = {sx, sy, sz, 0.f}; red[wv] = t; }
  }
#pragma unroll
  for (int r = 0; r < NPR; r++) {
    const int i = tid + r * 1024;
    if (i < npair) {
      u4 t;
      t.x = pw[r][0]; t.y = pw[r][1] & 0xffffu;
      t.z = __builtin_amdgcn_alignbit(pw[r][2], pw[r][1], 16); t.w = pw[r][2] >> 16;
      *(u4*)(tab + 2 * i) = t;
    }
  }
  if ((N & 1) && tid == 0) {
    const u32 n0 = N - 1;
    u2 t = {(u32)pos[3 * n0] | ((u32)pos[3 * n0 + 1] << 16), (u32)pos[3 * n0 + 2]};
    tab[n0] = t;
  }
  TS(0, 1);
  bar_lds();
  TS(0, 2);
  float sx, sy, sz = 0.f;
  {
    f4 t = red[lane & 15];
    sx = rdlanef(row_scanf(t.x), 15) * (1.f / 2048.f); sy = rdlanef(row_scanf(t.y), 15) * (1.f / 2048.f);
    if (CS == 1) sz = rdlanef(row_scanf(t.z), 15) * (1.f / 2048.f);
  }
  if (p == 0 && tid == 0) { split[0] = sx; split[1] = sy; split[2] = sz; }
  u32 rk[4], rxy[4], rz[4];
  u2 ta[4], tb[4];
#pragma unroll
  for (int u = 0; u < 4; u++) { ta[u] = tab[sI[u]]; tb[u] = tab[dI[u]]; }
#pragma unroll
  for (int u = 0; u < 4; u++) {
    const u2 a = ta[u], b = tb[u];
    const float bx = lo16(b.x), by = hi16(b.x);
    const float rx = lo16(a.x) - bx, ry = hi16(a.x) - by, rzz = lo16(a.y) - lo16(b.y);
    const float d2 = rx * rx + ry * ry + rzz * rzz;
    const bool cand = (e0 + u < E) && !(d2 >= 25.25f && d2 <= 3.0e38f);
    rk[u] = 0xffffffffu;
    if (cand) {
      const u32 cell = quadrant(bx, by, lo16(b.y), sx, sy, sz) * JG + __umulhi((u32)dI[u], magicM);
      rk[u] = atomicAdd(&hist[cell], 1u) | (cell << 16);
    }
    rxy[u] = pkbf(rx, ry); rz[u] = pkbf(rzz, 0.f);
  }
  bar_lds();
  TS(0, 3);
  if (wv == 0) {
    constexpr int CPL = NCELL / 64;
    u32 c[CPL], o[CPL], s = 0;
#pragma unroll
    for (int i = 0; i < CPL; i += 4) { u4 t = *(const u4*)(hist + lane * CPL + i); c[i] = t.x; c[i + 1] = t.y; c[i + 2] = t.z; c[i + 3] = t.w; }
#pragma unroll
    for (int i = 0; i < CPL; i++) s += c[i] > SLOTK ? c[i] - SLOTK : 0u;
    u32 ex = wave_scan(s) - s;
#pragma unroll
    for (int i = 0; i < CPL; i++) { o[i] = ex; ex += c[i] > SLOTK ? c[i] - SLOTK : 0u; }
#pragma unroll
    for (int i = 0; i < CPL; i += 4) { u4 t = {o[i], o[i + 1], o[i + 2], o[i + 3]}; *(u4*)(hist + lane * CPL + i) = t; }
#pragma unroll
    for (int i = 0; i < CPL; i++) desc[(lane * CPL + i) * P + p] = c[i] | (o[i] << 16);
  }
#pragma unroll
  for (int u = 0; u < 4; u++) {
    if (rk[u] != 0xffffffffu && (rk[u] & 0xffffu) < SLOTK) {
      u4 t = {(u32)sI[u], (u32)dI[u], rxy[u], rz[u]};
      rec[(size_t)(((rk[u] >> 16) * P + p) * SLOTK + (rk[u] & 0xffffu))] = t;
    }
  }
  bool anyo = false;
#pragma unroll
  for (int u = 0; u < 4; u++) anyo |= rk[u] != 0xffffffffu && (rk[u] & 0xffffu) >= SLOTK;
  bar_lds();
  TS(0, 4);
  if (anyo) {
#pragma unroll
    for (int u = 0; u < 4; u++) {
      if (rk[u] != 0xffffffffu && (rk[u] & 0xffffu) >= SLOTK) {
        const u32 idx = hist[rk[u] >> 16] + (rk[u] & 0xffffu) - SLOTK;
        u4 t = {(u32)sI[u], (u32)dI[u], rxy[u], rz[u]};
        ovf[(size_t)p * CHUNK + idx] = t;
      }
    }
  }
  TS(0, 5);
  TSDRAIN;
  TS(0, 6);
}

constexpr int HP = 272;
constexpr int PP = 784;
#define PHI_AUX 0
DI s8 as_s8(u4 x) { return __builtin_bit_cast(s8, x); }

DI void mlp_role(char* lds, const u16* __restrict__ sv, const u16* __restrict__ w1, const u16* __restrict__ w2,
                 u16* __restrict__ phi, int N, int tile) {
  const int tid = threadIdx.x, lane = tid & 63, wv = __builtin_amdgcn_readfirstlane(tid >> 6);
  const int g = lane >> 4, c = lane & 15;
  const int n0 = tile * (RT * 16);
  TS(0, 0);
  char* sS = lds;
  char* sH = lds + RT * 16 * HP;
  const char* svb = (const char*)sv;
  u4 sreg[2];
#pragma unroll
  for (int r = 0; r < 2; r++) {
    const int i = min(tid + r * 1024, RT * 256 - 1), row = i >> 4, ch = i & 15;
    sreg[r] = *(const u4*)(svb + (u32)(min(n0 + row, N - 1) * 1024 + ch * 16));
  }
  const char* wp = (const char*)((wv < 4) ? (w1 + 32 * wv) : (w2 + 32 * (wv - 4)));
  const int wld = (wv < 4) ? 256 : 768;
  u32 L[32];
  {
    const u32 wo = (u32)(8 * g * wld + 4 * c);
#pragma unroll
    for (int s = 0; s < 4; s++)
#pragma unroll
      for (int j = 0; j < 8; j++) L[s * 8 + j] = *(const u32*)(wp + (size_t)((32 * s + j) * wld) + wo);
  }
#pragma unroll
  for (int r = 0; r < 2; r++) {
    const int i = tid + r * 1024, row = i >> 4, ch = i & 15;
    if (row < RT * 16) *(u4*)(sS + row * HP + ch * 16) = sreg[r];
  }
  TS(0, 1);
  bar_lds();
  TS(0, 2);
  u32 warm = 0;
  u4 B[2][4];
  auto make_b = [&]() __attribute__((always_inline)) {
#pragma unroll
    for (int i = 0; i < 32; i += 8)
      asm volatile("" : "+v"(L[i]), "+v"(L[i + 1]), "+v"(L[i + 2]), "+v"(L[i + 3]), "+v"(L[i + 4]), "+v"(L[i + 5]), "+v"(L[i + 6]), "+v"(L[i + 7]));
#pragma unroll
    for (int s = 0; s < 4; s++)
#pragma unroll
      for (int i = 0; i < 4; i++) {
        B[0][s][i] = __builtin_amdgcn_perm(L[s * 8 + 2 * i + 1], L[s * 8 + 2 * i], 0x05040100u);
        B[1][s][i] = __builtin_amdgcn_perm(L[s * 8 + 2 * i + 1], L[s * 8 + 2 * i], 0x07060302u);
      }
  };
  if (wv < 4) {
    make_b();
    f4 a0[RT], a1[RT];
#pragma unroll
    for (int rt = 0; rt < RT; rt++) { f4 z = {0, 0, 0, 0}; a0[rt] = z; a1[rt] = z; }
#pragma unroll
    for (int s = 0; s < 4; s++)
#pragma unroll
      for (int rt = 0; rt < RT; rt++) {
        u4 A = *(const u4*)(sS + (rt * 16 + c) * HP + (32 * s + 8 * g) * 2);
        a0[rt] = __builtin_amdgcn_mfma_f32_16x16x32_bf16(as_s8(A), as_s8(B[0][s]), a0[rt], 0, 0, 0);
        a1[rt] = __builtin_amdgcn_mfma_f32_16x16x32_bf16(as_s8(A), as_s8(B[1][s]), a1[rt], 0, 0, 0);
      }
#pragma unroll
    for (int rt = 0; rt < RT; rt++)
#pragma unroll
      for (int r = 0; r < 4; r++) *(u32*)(sH + (rt * 16 + 4 * g + r) * HP + (32 * wv + 2 * c) * 2) = pkbf(a0[rt][r], a1[rt][r]);
  }
  bar_lds();
#pragma unroll
  for (int k = 0; k < (RT * 16 * 64 + 1023) / 1024; k++) {
    const int i = tid + k * 1024;
    if (i < RT * 16 * 64) {
      u32* hp = (u32*)(sH + (i >> 6) * HP + (i & 63) * 4);
      const u32 xr = *hp;
      const float x0 = lo16(xr), x1 = hi16(xr);
      const float h0 = x0 * __builtin_amdgcn_rcpf(1.f + __builtin_amdgcn_exp2f(x0 * -1.4426950408889634f));
      const float h1 = x1 * __builtin_amdgcn_rcpf(1.f + __builtin_amdgcn_exp2f(x1 * -1.4426950408889634f));
      *hp = pkbf(h0, h1);
    }
  }
  TS(0, 3);
  bar_lds();
  TSW(0, 4, 4);
  if (wv >= 4) {
    make_b();
    const rsrc_t rp = mk_rsrc(phi, (u32)N * 1024u);
    const u32 oo = (u32)((n0 + 4 * g) * 1024 + 64 * (wv - 4) + 4 * c);
#pragma unroll
    for (int rt = 0; rt < RT; rt++) {
      f4 a0 = {0, 0, 0, 0}, a1 = {0, 0, 0, 0};
#pragma unroll
      for (int s = 0; s < 4; s++) {
        u4 A = *(const u4*)(sH + (rt * 16 + c) * HP + (32 * s + 8 * g) * 2);
        a0 = __builtin_amdgcn_mfma_f32_16x16x32_bf16(as_s8(A), as_s8(B[0][s]), a0, 0, 0, 0);
        a1 = __builtin_amdgcn_mfma_f32_16x16x32_bf16(as_s8(A), as_s8(B[1][s]), a1, 0, 0, 0);
      }
#pragma unroll
      for (int r = 0; r < 4; r++) __builtin_amdgcn_raw_buffer_store_b32(pkbf(a0[r], a1[r]), rp, oo + (rt * 16 + r) * 1024, 0, PHI_AUX);
    }
  }
  TSW(0, 5, 4);
  TSDRAIN;
  TSW(0, 6, 4);
  asm volatile("" ::"v"(warm));
}

template <int NPR, bool VEC>
__global__ __launch_bounds__(1024) void k_prep(const u16* __restrict__ sv, const u16* __restrict__ pos, const int* __restrict__ src,
                                               const int* __restrict__ dst, const u16* __restrict__ w1, const u16* __restrict__ w2,
                                               u16* __restrict__ phi, u4* __restrict__ rec, u4* __restrict__ ovf, u32* __restrict__ desc,
                                               float* __restrict__ split, int N, int E, int P, u32 magicM) {
  extern __shared__ __attribute__((aligned(16))) char lds[];
  if ((int)blockIdx.x < P) filter_role<NPR, VEC>(lds, pos, src, dst, rec, ovf, desc, split, N, E, P, magicM);
  else mlp_role(lds, sv, w1, w2, phi, N, blockIdx.x - P);
}

#define K2W 8
#define K2OCC 4
constexpr int NW = K2W;
constexpr int NT = NW * 64;
constexpr int CH = NT;
constexpr int SLOTS = CH + (CS == 2 ? MMAX : 0) + 8;
constexpr int LPC = 64 / CS;
constexpr int L_PA = 0;
constexpr int L_PB = L_PA + SLOTS * 16;
constexpr int L_RAW = L_PB;
constexpr int L_CNT = L_PB + SLOTS * 16;
constexpr int L_START = L_CNT + MMAX * 4;
constexpr int L_MISC = L_START + MMAX * 4;
constexpr int L_HT = L_MISC + 512;
constexpr int L_W = L_HT + 2 * NW * 1024 * (2 / CS);
constexpr int L_TOTAL = L_W + 6 * LPC * 8;

struct TrueT { static constexpr bool value = true; };
struct FalseT { static constexpr bool value = false; };
DI f4 mfma444(u2 a, u2 b, f4 c) {
  return __builtin_amdgcn_mfma_f32_4x4x4bf16_1k(__builtin_bit_cast(s4, a), __builtin_bit_cast(s4, b), c, 0, 0, 0);
}

DI void agg_range(char* lds, rsrc_t rphi, rsrc_t rsv, rsrc_t rres, rsrc_t rout, u32 lofs, u32 nb, int lane, int wv, int itb, int ite) {
  const int half = CS == 2 ? lane >> 5 : 0, lp = lane & (LPC - 1);
  u2 wt[3][2];
#pragma unroll
  for (int g = 0; g < 3; g++)
#pragma unroll
    for (int t = 0; t < 2; t++) wt[g][t] = *(const u2*)(lds + L_W + ((g * 2 + t) * LPC + lp) * 8);
  u2 ident;
  ident.x = (lane & 3) == 0 ? 0x00003f80u : ((lane & 3) == 1 ? 0x3f800000u : 0u);
  ident.y = (lane & 3) == 2 ? 0x00003f80u : ((lane & 3) == 3 ? 0x3f800000u : 0u);
  const f4 zero = {0, 0, 0, 0};
  f2 acc[4];
#pragma unroll
  for (int r = 0; r < 4; r++) { f2 z = {0.f, 0.f}; acc[r] = z; }
  const u32 olane = (CS == 2 ? half * 512u : 0u) + lofs;
  const char* pbl = lds + L_PB + half * 16 + itb * (CS * 16);
  const char* pal = lds + L_PA + half * 16 + (itb + (lane & 3)) * (CS * 16);
  constexpr int NO = 2 / CS;
  char* hl = lds + L_HT + (wv * 64 + lane) * (16 * NO);
  bool pend = itb > 0 && itb < ite && (__builtin_amdgcn_readfirstlane(*(const u32*)(lds + L_PB + (itb - 1) * (CS * 16) + 12)) & 1u) == 0;
  u32 headso = 0xffffffffu, lastw = 1u;
  for (int it = itb; it < ite; it += 4, pbl += 4 * CS * 16, pal += 4 * CS * 16) {
    u32 w[4];
#pragma unroll
    for (int u = 0; u < 4; u++) w[u] = *(const u32*)(pbl + u * CS * 16 + 12);
    const u4 am = *(const u4*)pal;
    u2 b[4][3];
    u32 sw[4], q[4][4 / CS];
#pragma unroll
    for (int u = 0; u < 4; u++) {
      sw[u] = __builtin_amdgcn_readfirstlane(w[u]);
      if (u == 0 || it + u < ite) {
        const u32 vo = (w[u] & 0xfffffc00u) | lofs;
        b[u][0].x = bld(rphi, vo, 0);
        b[u][0].y = bld(rphi, vo + 256, 0);
        b[u][1].x = bld(rphi, vo + 512, 0);
        b[u][1].y = bld(rsv, vo + 256, 0);
        b[u][2].x = bld(rsv, vo + 512, 0);
        b[u][2].y = bld(rsv, vo + 768, 0);
        if (sw[u] & 1u) {
          const u32 so = (nb + ((sw[u] >> 1) & 0x1ffu)) << 10;
#pragma unroll
          for (int r = 0; r < 4 / CS; r++) q[u][r] = bld(rres, olane + r * 256, so);
        }
      }
    }
    asm volatile("" ::: "memory");
    f4 wf[3][2];
#pragma unroll
    for (int g = 0; g < 3; g++)
#pragma unroll
      for (int t = 0; t < 2; t++) {
        u2 ah = {am.x, am.y}, al = {am.z, am.w};
        wf[g][t] = mfma444(al, wt[g][t], mfma444(ah, wt[g][t], zero));
      }
#pragma unroll
    for (int u = 0; u < 4; u++) {
      if (it + u < ite) {
        const u4 rb = *(const u4*)(pbl + u * CS * 16);
        const f4 U0 = mfma444(ident, b[u][0], zero), U1 = mfma444(ident, b[u][1], zero), U2 = mfma444(ident, b[u][2], zero);
        const float r0 = __uint_as_float(rb.x), r1 = __uint_as_float(rb.y), r2 = __uint_as_float(rb.z);
        acc[0].x = __builtin_fmaf(U0[0], wf[0][0][u], acc[0].x);
        acc[0].y = __builtin_fmaf(U0[1], wf[0][1][u], acc[0].y);
        const float mv0 = U0[2] * wf[1][0][u], mv1 = U0[3] * wf[1][1][u];
        const float mr0 = U1[0] * wf[2][0][u], mr1 = U1[1] * wf[2][1][u];
        acc[1].x = __builtin_fmaf(mv0, U1[2], acc[1].x); acc[1].y = __builtin_fmaf(mv1, U1[3], acc[1].y);
        acc[2].x = __builtin_fmaf(mv0, U2[0], acc[2].x); acc[2].y = __builtin_fmaf(mv1, U2[1], acc[2].y);
        acc[3].x = __builtin_fmaf(mv0, U2[2], acc[3].x); acc[3].y = __builtin_fmaf(mv1, U2[3], acc[3].y);
        acc[1].x = __builtin_fmaf(mr0, r0, acc[1].x); acc[1].y = __builtin_fmaf(mr1, r0, acc[1].y);
        acc[2].x = __builtin_fmaf(mr0, r1, acc[2].x); acc[2].y = __builtin_fmaf(mr1, r1, acc[2].y);
        acc[3].x = __builtin_fmaf(mr0, r2, acc[3].x); acc[3].y = __builtin_fmaf(mr1, r2, acc[3].y);
        lastw = sw[u];
        if (sw[u] & 1u) {
          const u32 so = (nb + ((sw[u] >> 1) & 0x1ffu)) << 10;
          f4 o[NO];
          if (CS == 2) {
#pragma unroll
            for (int r = 0; r < 2; r++) {
              auto x = __builtin_amdgcn_permlane32_swap(__float_as_uint(acc[r].x), __float_as_uint(acc[r + 2].x), false, false);
              auto y = __builtin_amdgcn_permlane32_swap(__float_as_uint(acc[r].y), __float_as_uint(acc[r + 2].y), false, false);
              o[0][2 * r] = __uint_as_float(x[0]) + __uint_as_float(x[1]);
              o[0][2 * r + 1] = __uint_as_float(y[0]) + __uint_as_float(y[1]);
            }
          } else {
#pragma unroll
            for (int r = 0; r < NO; r++) { f4 t = {acc[2 * r].x, acc[2 * r].y, acc[2 * r + 1].x, acc[2 * r + 1].y}; o[r] = t; }
          }
#pragma unroll
          for (int r = 0; r < NO; r++) { u2 qq = {q[u][(2 * r) % (4 / CS)], q[u][(2 * r + 1) % (4 / CS)]}; o[r] = mfma444(ident, qq, o[r]); }
          if (pend) {
#pragma unroll
            for (int r = 0; r < NO; r++) *(f4*)(hl + r * 16) = o[r];
            headso = so; pend = false;
          } else {
#pragma unroll
            for (int r = 0; r < NO; r++) {
              bst(pkbf(o[r][0], o[r][1]), rout, olane + r * 512, so);
              bst(pkbf(o[r][2], o[r][3]), rout, olane + r * 512 + 256, so);
            }
          }
#pragma unroll
          for (int r = 0; r < 4; r++) { f2 z = {0.f, 0.f}; acc[r] = z; }
        }
      }
    }
  }
  const bool tail = (lastw & 1u) == 0;
  if (tail) {
    f4 o[NO];
    if (CS == 2) {
#pragma unroll
      for (int r = 0; r < 2; r++) {
        auto x = __builtin_amdgcn_permlane32_swap(__float_as_uint(acc[r].x), __float_as_uint(acc[r + 2].x), false, false);
        auto y = __builtin_amdgcn_permlane32_swap(__float_as_uint(acc[r].y), __float_as_uint(acc[r + 2].y), false, false);
        o[0][2 * r] = __uint_as_float(x[0]) + __uint_as_float(x[1]);
        o[0][2 * r + 1] = __uint_as_float(y[0]) + __uint_as_float(y[1]);
      }
    } else {
#pragma unroll
      for (int r = 0; r < NO; r++) { f4 t = {acc[2 * r].x, acc[2 * r].y, acc[2 * r + 1].x, acc[2 * r + 1].y}; o[r] = t; }
    }
#pragma unroll
    for (int r = 0; r < NO; r++) *(f4*)(hl + NW * 64 * 16 * NO + r * 16) = o[r];
  }
  if (lane == 0) *(u32*)(lds + L_MISC + (32 + wv) * 4) = (tail ? 1u : 0u) | ((pend || itb >= ite) ? 2u : 0u);
  bar_lds();
  if (headso != 0xffffffffu) {
    f4 o[NO];
#pragma unroll
    for (int r = 0; r < NO; r++) o[r] = *(const f4*)(hl + r * 16);
    for (int k = wv - 1; k >= 0; k--) {
      const u32 st = __builtin_amdgcn_readfirstlane(*(const u32*)(lds + L_MISC + (32 + k) * 4));
      if (st & 1u) {
#pragma unroll
        for (int r = 0; r < NO; r++) o[r] += *(const f4*)(lds + L_HT + ((NW + k) * 64 + lane) * (16 * NO) + r * 16);
      }
      if (!(st & 2u)) break;
    }
#pragma unroll
    for (int r = 0; r < NO; r++) {
      bst(pkbf(o[r][0], o[r][1]), rout, olane + r * 512, headso);
      bst(pkbf(o[r][2], o[r][3]), rout, olane + r * 512 + 256, headso);
    }
  }
}

NOPK __global__ __launch_bounds__(NT, K2OCC) void k_agg(const u16* __restrict__ sv, const u16* __restrict__ pos, const u16* __restrict__ freqs,
                                                        const u16* __restrict__ wr, const u16* __restrict__ phi, const u4* __restrict__ rec,
                                                        const u4* __restrict__ ovf, const u32* __restrict__ desc,
                                                        const float* __restrict__ split, u16* out, int N, int P, int M) {
  __shared__ __attribute__((aligned(16))) char lds[L_TOTAL];
  const int tid = threadIdx.x, lane = tid & 63, wv = __builtin_amdgcn_readfirstlane(tid >> 6);
  const int label = blockIdx.x & 7, q = CS == 2 ? label >> 1 : label, h = CS == 2 ? label & 1 : 0, j = blockIdx.x >> 3;
  const int cell = q * JG + j, nb = j * M;
  const int half = CS == 2 ? lane >> 5 : 0, lp = lane & (LPC - 1);
  u32* cnt = (u32*)(lds + L_CNT);
  u32* start = (u32*)(lds + L_START);
  u32* misc = (u32*)(lds + L_MISC);
  u4* raw = (u4*)(lds + L_RAW);
  u4* pa = (u4*)(lds + L_PA);
  u4* pb = (u4*)(lds + L_PB);
  const char* svb = (const char*)sv;
  const int nmw = (M + 63) >> 6;
  constexpr int RND = (PMAX * SLOTK + NT - 1) / NT;

  TS(1, 0);
  u32 dsc[RND];
  u4 pre[RND];
#pragma unroll
  for (int i = 0; i < RND; i++) {
    const u32 sl = min(tid + i * NT, P * SLOTK - 1);
    dsc[i] = desc[cell * P + (sl / SLOTK)];
    pre[i] = rec[(u32)cell * (u32)(P * SLOTK) + sl];
  }
  const u32 nc = min(nb + tid, N - 1);
  const u16* pp = (const u16*)((const char*)pos + nc * 6u);
  const u32 px = pp[0], py = pp[1], pz = CS == 1 ? pp[2] : 0u;
  constexpr int NWE = 6 * LPC;
  static_assert(NWE <= NT, "W staging");
  u32 wk[4];
  {
    const int we = min(tid, NWE - 1), gt = we / LPC, l = we % LPC;
    const u16* wp = wr + (gt >> 1) * 128 + h * 64 + 2 * l + (gt & 1);
#pragma unroll
    for (int k = 0; k < 4; k++) wk[k] = wp[k * 384];
  }
  const float sx = split[0], sy = split[1], sz = CS == 1 ? split[2] : 0.f;
  const u32 fq01 = *(const u32*)freqs, fq23 = *(const u32*)(freqs + 2);
  for (int i = tid; i < MMAX; i += NT) cnt[i] = 0;
  if (tid < 2) misc[tid] = 0;
  bar_lds();

  u32 pendm = 0;
  {
    unsigned long long vm[RND], vm2[RND];
    u4 pre2[RND];
    bool ovfl = false;
#pragma unroll
    for (int i = 0; i < RND; i++) {
      const int sl = tid + i * NT;
      const u32 c = sl < P * SLOTK ? dsc[i] & 0xffffu : 0u, k = sl % SLOTK;
      const bool v2 = k + SLOTK < c;
      vm[i] = ballot64(k < c);
      vm2[i] = ballot64(v2);
      if (vm2[i]) pre2[i] = ovf[v2 ? (u32)(sl / SLOTK) * CHUNK + (dsc[i] >> 16) + k : 0u];
      ovfl |= c > 2 * SLOTK;
    }
    u32 tot = 0;
#pragma unroll
    for (int i = 0; i < RND; i++) tot += __popcll(vm[i]) + __popcll(vm2[i]);
    u32 base = 0;
    if (lane == 0) base = atomicAdd(&misc[0], tot);
    if (ballot64(ovfl) != 0 && lane == 0) misc[1] = 1;
    base = __builtin_amdgcn_readfirstlane(base);
#pragma unroll
    for (int i = 0; i < RND; i++) {
      const u32 pos = base + __builtin_amdgcn_mbcnt_hi((u32)(vm[i] >> 32), __builtin_amdgcn_mbcnt_lo((u32)vm[i], 0));
      base += __popcll(vm[i]);
      if (vm[i] >> lane & 1) { if (pos < CH) raw[pos] = pre[i]; else pendm |= 1u << i; }
    }
#pragma unroll
    for (int i = 0; i < RND; i++) {
      if (vm2[i]) {
        const u32 pos = base + __builtin_amdgcn_mbcnt_hi((u32)(vm2[i] >> 32), __builtin_amdgcn_mbcnt_lo((u32)vm2[i], 0));
        base += __popcll(vm2[i]);
        if (vm2[i] >> lane & 1) { if (pos < CH) raw[pos] = pre2[i]; else pendm |= 1u << (RND + i); }
      }
    }
  }
  const bool member = tid < M && nb + tid < N && quadrant(lo16(px), lo16(py), lo16(pz), sx, sy, sz) == (u32)q;
  u32 touch[8 / CS];
#pragma unroll
  for (int i = 0; i < 8 / CS; i++) touch[i] = *(const u32*)(svb + (member ? nc * 1024u + (CS == 2 ? i * 256u + h * 128u : i * 128u) : 0u));
  if (tid < NWE) { u2 t = {wk[0] | (wk[1] << 16), wk[2] | (wk[3] << 16)}; *(u2*)(lds + L_W + tid * 8) = t; }
  const u32 lofs = h * 128 + lp * 4;
  const rsrc_t rphi = mk_rsrc(phi, (u32)N * 1024u), rsv = mk_rsrc(sv, (u32)N * 1024u), rout = mk_rsrc(out, (u32)N * 1024u);
  TS(1, 1);
  bar_lds();
  TS(1, 2);
  u32 nslot = __builtin_amdgcn_readfirstlane(misc[0]);
  const bool anyovf = __builtin_amdgcn_readfirstlane(misc[1]) != 0;
  u32 total = nslot;
  auto stage_ovf = [&](u32 c0) __attribute__((always_inline)) -> u32 {
    u32 pos = nslot;
    for (int p0 = 0; p0 < P; p0 += 64) {
      const u32 d = p0 + lane < P ? desc[cell * P + p0 + lane] : 0u;
      const u32 c = d & 0xffffu, n = c > 2 * SLOTK ? c - 2 * SLOTK : 0u;
      const u32 inc = wave_scan(n);
      if (wv == 0) {
        const u32 my = pos + inc - n;
        for (u32 k = 0; k < n; k++)
          if (my + k >= c0 && my + k < c0 + CH) raw[my + k - c0] = ovf[(size_t)(p0 + lane) * CHUNK + (d >> 16) + SLOTK + k];
      }
      pos += __builtin_amdgcn_readlane(inc, 63);
    }
    return pos;
  };
  if (anyovf) { total = stage_ovf(0u); bar_lds(); }

  auto process = [&](auto first_tag, u32 nrec) __attribute__((always_inline)) {
    constexpr bool FIRST = decltype(first_tag)::value;
    bool act = false; u32 loc = 0, rnk = 0;
    u4 ca = {0, 0, 0, 0}, cb = {0, 0, 0, 0};
    if ((u32)(wv * 64) < nrec) {
      const float frq[4] = {lo16(fq01), hi16(fq01), lo16(fq23), hi16(fq23)};
      const u4 t = raw[tid];
      loc = t.y - nb;
      const float rx = lo16(t.z), ry = hi16(t.z), rz = lo16(t.w);
      const u32 s01 = pkbf(rx * rx, ry * ry);
      float ss = lo16(s01) + hi16(s01) + rbf(rz * rz);
      ss = rbf(ss); ss = rbf(ss + 1e-8f);
      const float dd = rbf(__builtin_amdgcn_sqrtf(ss));
      act = (u32)tid < nrec && !(dd >= 5.0f && dd <= 3.0e38f);
      if (act) rnk = atomicAdd(&cnt[loc], 1u);
      const float id = __builtin_amdgcn_rcpf(dd);
      const u32 rh01 = pkbf(rx * id, ry * id);
      cb.x = rh01 << 16; cb.y = rh01 & 0xffff0000u; cb.z = pkbf(0.f, rz * id) & 0xffff0000u; cb.w = t.x;
      float fc = rbf(3.14159265358979f * dd);
      fc = rbf(fc * 0.2f);
      fc = rbf(__builtin_amdgcn_cosf(__builtin_amdgcn_fractf(fc * 0.15915494309189535f)));
      fc = 0.5f * rbf(fc + 1.0f);
      if (!(dd < 5.0f)) fc = 0.0f * dd;
      float a[4];
#pragma unroll
      for (int k = 0; k < 4; k += 2) {
        const u32 x = pkbf(dd * frq[k], dd * frq[k + 1]);
        const u32 y = pkbf(__builtin_amdgcn_sinf(__builtin_amdgcn_fractf(lo16(x) * 0.15915494309189535f)),
                           __builtin_amdgcn_sinf(__builtin_amdgcn_fractf(hi16(x) * 0.15915494309189535f)));
        const u32 z = pkbf(lo16(y) * id, hi16(y) * id);
        a[k] = lo16(z) * fc; a[k + 1] = hi16(z) * fc;
      }
      ca.x = pkbf(a[0], a[1]); ca.y = pkbf(a[2], a[3]);
      ca.z = pkbf(a[0] - lo16(ca.x), a[1] - hi16(ca.x)); ca.w = pkbf(a[2] - lo16(ca.y), a[3] - hi16(ca.y));
    }
    bar_lds();
    TS(1, 4);
    int T;
    {
      int s4[4];
      const u4 c4 = *(const u4*)(cnt + lane * 4);
      u32 it[4] = {c4.x, c4.y, c4.z, c4.w}, sum = 0;
#pragma unroll
      for (int i = 0; i < 4; i++) { if (CS == 2) it[i] = (it[i] + 1) >> 1; sum += it[i]; }
      const u32 inc = wave_scan(sum);
      T = (int)__builtin_amdgcn_readlane(inc, 63);
      u32 ex = inc - sum;
#pragma unroll
      for (int i = 0; i < 4; i++) { s4[i] = (int)ex; ex += it[i]; }
      u4 sv4 = {(u32)s4[0], (u32)s4[1], (u32)s4[2], (u32)s4[3]};
      *(u4*)(start + lane * 4) = sv4;
    }
    if (act) {
      const u32 st = start[loc], sl = CS * st + rnk;
      const u32 cc = cnt[loc];
      const u32 last = CS == 2 ? ((rnk | 1u) + 1 >= cc) : (rnk + 1 == cc);
      cb.w = (cb.w << 10) | (loc << 1) | last;
      pa[sl] = ca; pb[sl] = cb;
      if (CS == 2 && rnk + 1 == cc && (cc & 1)) { u4 z = {0, 0, 0, 0}; pa[sl + 1] = z; u4 t = {0, 0, 0, cb.w}; pb[sl + 1] = t; }
    }
    bar_lds();
    TS(1, 5);
    const int itb = (wv * T) / NW, ite = ((wv + 1) * T) / NW;
    if (FIRST) {
#pragma unroll
      for (int i = 0; i < 8 / CS; i++) asm volatile("" ::"v"(touch[i]));
    }
    TS(1, 6);
    agg_range(lds, rphi, rsv, FIRST ? rsv : rout, rout, lofs, nb, lane, wv, itb, ite);
    TS(1, 7);
  };
  TS(1, 3);
  process(TrueT(), min(total, (u32)CH));
  if (wv < nmw) {
    unsigned long long em = ballot64(member && cnt[min(tid, MMAX - 1)] == 0);
    while (em) {
      const int l = __builtin_ctzll(em); em &= em - 1;
      const size_t go = (size_t)(nb + wv * 64 + l) * 1024 + (CS == 2 ? half * 512u : 0u) + lofs;
#pragma unroll
      for (int r = 0; r < 4 / CS; r++) *(u32*)((char*)out + go + r * 256) = *(const u32*)(svb + go + r * 256);
    }
  }
  if (total > CH) {
    u32 left = nslot > CH ? nslot - CH : 0u;
    while (left) {
      bar_all();
      for (int i = tid; i < MMAX; i += NT) cnt[i] = 0;
      if (tid == 0) misc[0] = 0;
      bar_lds();
#pragma unroll
      for (int i = 0; i < 2 * RND; i++) {
        const unsigned long long m = ballot64(pendm >> i & 1);
        if (m) {
          u32 base = 0;
          if (lane == 0) base = atomicAdd(&misc[0], (u32)__popcll(m));
          base = __builtin_amdgcn_readfirstlane(base);
          const u32 pos = base + __builtin_amdgcn_mbcnt_hi((u32)(m >> 32), __builtin_amdgcn_mbcnt_lo((u32)m, 0));
          if ((pendm >> i & 1) && pos < CH) {
            const int sl = tid + (i % RND) * NT;
            if (i < RND) raw[pos] = rec[(u32)cell * (u32)(P * SLOTK) + sl];
            else raw[pos] = ovf[(u32)(sl / SLOTK) * CHUNK + (desc[cell * P + sl / SLOTK] >> 16) + sl % SLOTK];
            pendm &= ~(1u << i);
          }
        }
      }
      bar_lds();
      const u32 n = min(left, (u32)CH);
      process(FalseT(), n);
      left -= n;
    }
    if (anyovf) {
      const u32 lo = max(nslot, (u32)CH);
      for (u32 c0 = lo; c0 < total; c0 += CH) {
        bar_all();
        for (int i = tid; i < MMAX; i += NT) cnt[i] = 0;
        stage_ovf(c0);
        bar_lds();
        process(FalseT(), min(total - c0, (u32)CH));
      }
    }
  }
  TS(1, 8);
  TSDRAIN;
  TS(1, 9);
}

__global__ void fb_mlp(const u16* sv, const u16* w1, const u16* w2, u16* phi) {
  __shared__ float s[128], hh[128];
  int n = blockIdx.x, t = threadIdx.x;
  s[t] = lo16(sv[(size_t)n * 512 + t]);
  __syncthreads();
  float acc = 0.f;
  for (int k = 0; k < 128; k++) acc += s[k] * lo16(w1[k * 128 + t]);
  acc = rbf(acc);
  hh[t] = rbf(acc / (1.f + expf(-acc)));
  __syncthreads();
  for (int g = 0; g < 3; g++) {
    float a = 0.f;
    for (int k = 0; k < 128; k++) a += hh[k] * lo16(w2[k * 384 + g * 128 + t]);
    phi[(size_t)n * 384 + g * 128 + t] = (u16)pkbf(a, 0.f);
  }
}
__global__ void fb_edge(const u16* sv, const u16* pos, const int* src, const int* dst, const u16* freqs, const u16* wr,
                        const u16* phi, float* acc) {
  int e = blockIdx.x, t = threadIdx.x;
  size_t s = src[e], d = dst[e];
  float r[3], ss = 0.f;
  for (int x = 0; x < 3; x++) { r[x] = rbf(lo16(pos[s * 3 + x]) - lo16(pos[d * 3 + x])); ss += rbf(r[x] * r[x]); }
  ss = rbf(ss); ss = rbf(ss + 1e-8f);
  float dd = rbf(sqrtf(ss));
  if (dd >= 5.0f && dd <= 3.0e38f) return;
  float fc = rbf(3.14159265358979f * dd); fc = rbf(fc / 5.0f); fc = rbf(cosf(fc)); fc = rbf(fc + 1.0f); fc = rbf(0.5f * fc);
  if (!(dd < 5.0f)) fc = 0.0f * dd;
  float wf[3] = {0.f, 0.f, 0.f};
  for (int k = 0; k < 4; k++) {
    float a = rbf(rbf(sinf(rbf(dd * lo16(freqs[k])))) / dd) * fc;
    for (int g = 0; g < 3; g++) wf[g] += a * lo16(wr[k * 384 + g * 128 + t]);
  }
  atomicAdd(&acc[d * 512 + t], lo16(phi[s * 384 + t]) * wf[0]);
  float mv = lo16(phi[s * 384 + 128 + t]) * wf[1], mr = lo16(phi[s * 384 + 256 + t]) * wf[2];
  for (int x = 0; x < 3; x++) atomicAdd(&acc[d * 512 + 128 + x * 128 + t], mv * lo16(sv[s * 512 + 128 + x * 128 + t]) + mr * rbf(r[x] / dd));
}
__global__ void fb_fin(const u16* sv, const float* acc, u16* out, size_t n) {
  size_t i = blockIdx.x * (size_t)256 + threadIdx.x;
  if (i < n) out[i] = (u16)pkbf(lo16(sv[i]) + acc[i], 0.f);
}

extern "C" void kernel_launch(void* const* d_in, const int* in_sizes, int n_in, void* d_out, int out_size, void* d_ws,
                              size_t ws_size, hipStream_t stream) {
  const u16* sv = (const u16*)d_in[0];
  const u16* pos = (const u16*)d_in[1];
  const int* src = (const int*)d_in[2];
  const int* dst = (const int*)d_in[3];
  const u16* freqs = (const u16*)d_in[4];
  const u16* w1 = (const u16*)d_in[5];
  const u16* w2 = (const u16*)d_in[6];
  const u16* wr = (const u16*)d_in[7];
  u16* out = (u16*)d_out;
  const int N = in_sizes[0] / 512, E = in_sizes[2];
  const int P = (E + CHUNK - 1) / CHUNK;
  const int M = (N + JG - 1) / JG;
  char* ws = (char*)d_ws;
  u16* phi = (u16*)ws;
  size_t off = ((size_t)N * 1024 + 255) & ~(size_t)255;
  if (N >= 2 && E >= 4 && M <= MMAX && M <= NT && N <= 20000 && P <= PMAX) {
    u4* rec = (u4*)(ws + off); off += (size_t)NCELL * P * SLOTK * 16;
    u4* ovf = (u4*)(ws + off); off += (size_t)P * CHUNK * 16;
    u32* desc = (u32*)(ws + off); off += (((size_t)P * NCELL * 4) + 255) & ~(size_t)255;
    float* split = (float*)(ws + off);
    const u32 magicM = (u32)(0x100000000ull / (u32)M) + 1u;
    const int nmlp = (N + RT * 16 - 1) / (RT * 16);
    size_t lds1 = (size_t)((N + 1) & ~1) * 8 + NCELL * 4 + 256;
    const size_t ldsm = 2 * RT * 16 * HP + RT * 16 * PP;
    if (lds1 < ldsm) lds1 = ldsm;
#define LAUNCH_PREP(NPR_, VEC_) do { \
      (void)hipFuncSetAttribute((const void*)k_prep<NPR_, VEC_>, hipFuncAttributeMaxDynamicSharedMemorySize, 160 * 1024); \
      k_prep<NPR_, VEC_><<<P + nmlp, 1024, lds1, stream>>>(sv, pos, src, dst, w1, w2, phi, rec, ovf, desc, split, N, E, P, magicM); } while (0)
    if (N <= 7 * 2048 && (E & 3) == 0) LAUNCH_PREP(7, true);
    else if ((E & 3) == 0) LAUNCH_PREP(10, true);
    else LAUNCH_PREP(10, false);
    k_agg<<<8 * JG, NT, 0, stream>>>(sv, pos, freqs, wr, phi, rec, ovf, desc, split, out, N, P, M);
  } else {
    float* acc = (float*)(ws + off);
    (void)hipMemsetAsync(acc, 0, (size_t)N * 512 * 4, stream);
    fb_mlp<<<N, 128, 0, stream>>>(sv, w1, w2, phi);
    if (E > 0) fb_edge<<<E, 128, 0, stream>>>(sv, pos, src, dst, freqs, wr, phi, acc);
    fb_fin<<<(unsigned)(((size_t)N * 512 + 255) / 256), 256, 0, stream>>>(sv, acc, out, (size_t)N * 512);
  }
}
```

```cpp
#include <hip/hip_runtime.h>
#include <stdint.h>

typedef unsigned short u16;
typedef unsigned int u32;
typedef float f2 __attribute__((ext_vector_type(2)));
typedef float f4 __attribute__((ext_vector_type(4)));
typedef u32 u2 __attribute__((ext_vector_type(2)));
typedef u32 u4 __attribute__((ext_vector_type(4)));
typedef short s4 __attribute__((ext_vector_type(4)));
typedef short s8 __attribute__((ext_vector_type(8)));
typedef __bf16 bf2 __attribute__((ext_vector_type(2)));

#define DI __device__ __forceinline__
#define INL __attribute__((always_inline))
#if defined(__HIP_DEVICE_COMPILE__)
#define NOPK __attribute__((target("no-packed-fp32-ops")))
#else
#define NOPK
#endif

constexpr int NQ = 4;
constexpr int JG = 64;
constexpr int NCELL = NQ * JG;
constexpr int CHUNK = 4096;
constexpr int RT = 5;
constexpr int MPL = 4;
constexpr int MMAX = MPL * 64;
constexpr int PMAX = 128;
constexpr int SLOTK = 8;
constexpr int AUX_NT = 2, AUX_SC1 = 16;
constexpr u32 OOB = 0x40000000u;

DI float lo16(u32 x) { return __uint_as_float(x << 16); }
DI float hi16(u32 x) { return __uint_as_float(x & 0xffff0000u); }
DI u32 pkbf(float a, float b) { f2 f = {a, b}; bf2 v = __builtin_convertvector(f, bf2); return __builtin_bit_cast(u32, v); }
DI float rbf(float a) { return lo16(pkbf(a, 0.f)); }

DI void bar_lds() {
  asm volatile("s_waitcnt lgkmcnt(0)" ::: "memory");
  __builtin_amdgcn_s_barrier();
  asm volatile("" ::: "memory");
}
DI void bar_all() {
  asm volatile("s_waitcnt vmcnt(0) lgkmcnt(0)" ::: "memory");
  __builtin_amdgcn_s_barrier();
  asm volatile("" ::: "memory");
}
DI unsigned long long ballot64(bool p) { return __builtin_amdgcn_ballot_w64(p); }
DI u32 mbcnt64(unsigned long long m) { return __builtin_amdgcn_mbcnt_hi((u32)(m >> 32), __builtin_amdgcn_mbcnt_lo((u32)m, 0)); }
typedef __amdgpu_buffer_rsrc_t rsrc_t;
DI rsrc_t mk_rsrc(const void* p, u32 bytes) { return __builtin_amdgcn_make_buffer_rsrc((void*)p, 0, bytes, 0x00020000); }
DI u32 bld(rsrc_t r, u32 vo, u32 so) { return __builtin_amdgcn_raw_buffer_load_b32(r, vo, so, 0); }
DI void bst(u32 v, rsrc_t r, u32 vo, u32 so) { __builtin_amdgcn_raw_buffer_store_b32(v, r, vo, so, AUX_NT); }

template <int CTRL, int RMASK> DI u32 dpp_mov(u32 x) { return (u32)__builtin_amdgcn_update_dpp(0, (int)x, CTRL, RMASK, 0xf, false); }
DI u32 wave_scan(u32 x) {
  x += dpp_mov<0x111, 0xf>(x);
  x += dpp_mov<0x112, 0xf>(x);
  x += dpp_mov<0x114, 0xf>(x);
  x += dpp_mov<0x118, 0xf>(x);
  x += dpp_mov<0x142, 0xa>(x);
  x += dpp_mov<0x143, 0xc>(x);
  return x;
}
template <int CTRL, int RMASK> DI float dpp_addf(float x) { return x + __uint_as_float(dpp_mov<CTRL, RMASK>(__float_as_uint(x))); }
DI float row_scanf(float x) {
  x = dpp_addf<0x111, 0xf>(x); x = dpp_addf<0x112, 0xf>(x); x = dpp_addf<0x114, 0xf>(x); x = dpp_addf<0x118, 0xf>(x);
  return x;
}
DI float wave_scanf(float x) {
  x = row_scanf(x); x = dpp_addf<0x142, 0xa>(x); x = dpp_addf<0x143, 0xc>(x);
  return x;
}
DI float rdlanef(float x, int l) { return __uint_as_float(__builtin_amdgcn_readlane(__float_as_uint(x), l)); }
DI u32 quadrant(float x, float y, float sx, float sy) { return (x > sx ? 1u : 0u) | (y > sy ? 2u : 0u); }

template <int NPR, bool VEC>
DI void filter_role(char* lds, const u16* __restrict__ sv, const u16* __restrict__ pos, const int* __restrict__ src, const int* __restrict__ dst,
                    u4* __restrict__ rec, u4* __restrict__ ovf, float* __restrict__ split, int N, int E, int P, u32 magicM) {
  const int tid = threadIdx.x, lane = tid & 63, wv = __builtin_amdgcn_readfirstlane(tid >> 6), p = blockIdx.x;
  u32* hist = (u32*)lds;
  u32* ooff = hist + NCELL;
  f4* red = (f4*)(lds + NCELL * 8);
  u4* img = (u4*)(lds + NCELL * 8 + 256);
  u2* tab = (u2*)(lds + NCELL * 8 + 256 + NCELL * SLOTK * 16);
  const int e0 = p * CHUNK + tid * 4;
  const int npair = N >> 1;
  const char* posb = (const char*)pos;
  u32 sm0, sm1, sm2;
  { const u32* t = (const u32*)(posb + (u32)(12 * (int)(((unsigned)tid * (unsigned)npair) >> 10))); sm0 = t[0]; sm1 = t[1]; sm2 = t[2]; }
  u32 pw[NPR][3];
#pragma unroll
  for (int r = 0; r < NPR; r++) {
    const u32* t = (const u32*)(posb + (u32)(12 * min(tid + r * 1024, npair - 1)));
    pw[r][0] = t[0]; pw[r][1] = t[1]; pw[r][2] = t[2];
  }
  int sI[4], dI[4];
  if (VEC) {
    const int ec = min(e0, E - 4);
    int4 a = *(const int4*)(src + ec), b = *(const int4*)(dst + ec);
    sI[0] = a.x; sI[1] = a.y; sI[2] = a.z; sI[3] = a.w;
    dI[0] = b.x; dI[1] = b.y; dI[2] = b.z; dI[3] = b.w;
  } else {
#pragma unroll
    for (int u = 0; u < 4; u++) { const int ec = min(e0 + u, E - 1); sI[u] = src[ec]; dI[u] = dst[ec]; }
  }
  if (tid < NCELL) hist[tid] = 0;
  {
    const float sx = wave_scanf(lo16(sm0) + hi16(sm1)), sy = wave_scanf(hi16(sm0) + lo16(sm2));
    if (lane == 63) { f4 t = {sx, sy, 0.f, 0.f}; red[wv] = t; }
  }
#pragma unroll
  for (int r = 0; r < NPR; r++) {
    const int i = tid + r * 1024;
    if (i < npair) {
      u4 t;
      t.x = pw[r][0]; t.y = pw[r][1] & 0xffffu;
      t.z = __builtin_amdgcn_alignbit(pw[r][2], pw[r][1], 16); t.w = pw[r][2] >> 16;
      *(u4*)(tab + 2 * i) = t;
    }
  }
  if ((N & 1) && tid == 0) {
    const u32 n0 = N - 1;
    u2 t = {(u32)pos[3 * n0] | ((u32)pos[3 * n0 + 1] << 16), (u32)pos[3 * n0 + 2]};
    tab[n0] = t;
  }
  bar_lds();
  u32 warm;
  {
    asm volatile("" : "+v"(sI[0]), "+v"(sI[1]), "+v"(sI[2]), "+v"(sI[3]), "+v"(dI[0]), "+v"(dI[1]), "+v"(dI[2]), "+v"(dI[3]));
    const u32 k = min((u32)(p * 1024 + tid), (u32)N * 6u - 1u), row = k / 6u, ln = k - row * 6u;
    warm = *(const u32*)((const char*)sv + (row * 1024u + 256u + ln * 128u));
  }
  float sx, sy;
  {
    const f4 t = red[lane & 15];
    sx = rdlanef(row_scanf(t.x), 15) * (1.f / 2048.f); sy = rdlanef(row_scanf(t.y), 15) * (1.f / 2048.f);
  }
  if (p == 0 && tid == 0) { split[0] = sx; split[1] = sy; }
  u32 rk[4], rxy[4], rz[4];
  u2 ta[4], tb[4];
#pragma unroll
  for (int u = 0; u < 4; u++) { ta[u] = tab[sI[u]]; tb[u] = tab[dI[u]]; }
#pragma unroll
  for (int u = 0; u < 4; u++) {
    const u2 a = ta[u], b = tb[u];
    const float bx = lo16(b.x), by = hi16(b.x);
    const float rx = lo16(a.x) - bx, ry = hi16(a.x) - by, rzz = lo16(a.y) - lo16(b.y);
    const float d2 = rx * rx + ry * ry + rzz * rzz;
    const bool cand = (e0 + u < E) && !(d2 >= 25.25f && d2 <= 3.0e38f);
    rk[u] = 0xffffffffu;
    if (cand) {
      const u32 cell = quadrant(bx, by, sx, sy) * JG + __umulhi((u32)dI[u], magicM);
      rk[u] = atomicAdd(&hist[cell], 1u) | (cell << 16);
    }
    rxy[u] = pkbf(rx, ry); rz[u] = pkbf(rzz, 0.f);
  }
  bar_lds();
  if (wv == 0) {
    constexpr int CPL = NCELL / 64;
    u32 c[CPL], s = 0;
#pragma unroll
    for (int i = 0; i < CPL; i++) c[i] = hist[lane * CPL + i];
#pragma unroll
    for (int i = 0; i < CPL; i++) s += c[i] > SLOTK ? c[i] - SLOTK : 0u;
    u32 ex = wave_scan(s) - s;
#pragma unroll
    for (int i = 0; i < CPL; i++) { ooff[lane * CPL + i] = ex; ex += c[i] > SLOTK ? c[i] - SLOTK : 0u; }
  }
  bool anyo = false;
#pragma unroll
  for (int u = 0; u < 4; u++) {
    if (rk[u] != 0xffffffffu && (rk[u] & 0xffffu) < SLOTK) {
      u4 t = {(u32)sI[u], (u32)dI[u], rxy[u], (rz[u] & 0xffffu) | (hist[rk[u] >> 16] << 16)};
      img[(rk[u] >> 16) * SLOTK + (rk[u] & 0xffffu)] = t;
    }
    anyo |= rk[u] != 0xffffffffu && (rk[u] & 0xffffu) >= SLOTK;
  }
  for (int i = tid; i < NCELL * SLOTK; i += 1024) {
    const u32 c = hist[i / SLOTK];
    if ((u32)(i % SLOTK) >= c) { u4 t = {0, 0, 0, c << 16}; img[i] = t; }
  }
  bar_lds();
  {
    const rsrc_t rr = mk_rsrc(rec, (u32)(NCELL * P * SLOTK) * 16u);
    for (int i = tid; i < NCELL * SLOTK; i += 1024) {
      u4 t = img[i];
      if ((t.w >> 16) > SLOTK) t.y |= ooff[i / SLOTK] << 16;
      __builtin_amdgcn_raw_buffer_store_b128(t, rr, (u32)((i / SLOTK) * P + p) * (SLOTK * 16u) + (i % SLOTK) * 16u, 0, 0);
    }
  }
  if (anyo) {
#pragma unroll
    for (int u = 0; u < 4; u++) {
      if (rk[u] != 0xffffffffu && (rk[u] & 0xffffu) >= SLOTK) {
        const u32 idx = ooff[rk[u] >> 16] + (rk[u] & 0xffffu) - SLOTK;
        u4 t = {(u32)sI[u], (u32)dI[u], rxy[u], rz[u]};
        ovf[(size_t)p * CHUNK + idx] = t;
      }
    }
  }
  asm volatile("" ::"v"(warm));
}

constexpr int HP = 272;
DI s8 as_s8(u4 x) { return __builtin_bit_cast(s8, x); }

DI void mlp_role(char* lds, const u16* __restrict__ sv, const u16* __restrict__ w1, const u16* __restrict__ w2,
                 u16* __restrict__ phi, int N, int tile) {
  const int tid = threadIdx.x, lane = tid & 63, wv = __builtin_amdgcn_readfirstlane(tid >> 6);
  const int g = lane >> 4, c = lane & 15;
  const int n0 = tile * (RT * 16);
  char* sS = lds;
  char* sH = lds + RT * 16 * HP;
  const char* svb = (const char*)sv;
  u4 sreg[2];
#pragma unroll
  for (int r = 0; r < 2; r++) {
    const int i = min(tid + r * 1024, RT * 256 - 1), row = i >> 4, ch = i & 15;
    sreg[r] = *(const u4*)(svb + (u32)(min(n0 + row, N - 1) * 1024 + ch * 16));
  }
  const char* wp = (const char*)((wv < 4) ? (w1 + 32 * wv) : (w2 + 32 * (wv - 4)));
  const int wld = (wv < 4) ? 256 : 768;
  u32 L[32];
  if (wv >= 4) __builtin_amdgcn_s_sleep(6);
  {
    const u32 wo = (u32)(8 * g * wld + 4 * c);
#pragma unroll
    for (int s = 0; s < 4; s++)
#pragma unroll
      for (int j = 0; j < 8; j++) L[s * 8 + j] = *(const u32*)(wp + (size_t)((32 * s + j) * wld) + wo);
  }
#pragma unroll
  for (int r = 0; r < 2; r++) {
    const int i = tid + r * 1024, row = i >> 4, ch = i & 15;
    if (row < RT * 16) *(u4*)(sS + row * HP + ch * 16) = sreg[r];
  }
  bar_lds();
  u4 B[2][4];
  auto make_b = [&]() INL {
#pragma unroll
    for (int i = 0; i < 32; i += 8)
      asm volatile("" : "+v"(L[i]), "+v"(L[i + 1]), "+v"(L[i + 2]), "+v"(L[i + 3]), "+v"(L[i + 4]), "+v"(L[i + 5]), "+v"(L[i + 6]), "+v"(L[i + 7]));
#pragma unroll
    for (int s = 0; s < 4; s++)
#pragma unroll
      for (int i = 0; i < 4; i++) {
        B[0][s][i] = __builtin_amdgcn_perm(L[s * 8 + 2 * i + 1], L[s * 8 + 2 * i], 0x05040100u);
        B[1][s][i] = __builtin_amdgcn_perm(L[s * 8 + 2 * i + 1], L[s * 8 + 2 * i], 0x07060302u);
      }
  };
  if (wv < 4) {
    make_b();
    f4 a0[RT], a1[RT];
#pragma unroll
    for (int rt = 0; rt < RT; rt++) { f4 z = {0, 0, 0, 0}; a0[rt] = z; a1[rt] = z; }
#pragma unroll
    for (int s = 0; s < 4; s++)
#pragma unroll
      for (int rt = 0; rt < RT; rt++) {
        u4 A = *(const u4*)(sS + (rt * 16 + c) * HP + (32 * s + 8 * g) * 2);
        a0[rt] = __builtin_amdgcn_mfma_f32_16x16x32_bf16(as_s8(A), as_s8(B[0][s]), a0[rt], 0, 0, 0);
        a1[rt] = __builtin_amdgcn_mfma_f32_16x16x32_bf16(as_s8(A), as_s8(B[1][s]), a1[rt], 0, 0, 0);
      }
#pragma unroll
    for (int rt = 0; rt < RT; rt++)
#pragma unroll
      for (int r = 0; r < 4; r++) *(u32*)(sH + (rt * 16 + 4 * g + r) * HP + (32 * wv + 2 * c) * 2) = pkbf(a0[rt][r], a1[rt][r]);
  }
  bar_lds();
  constexpr int NI = RT * 16 * 64 / 1024;
  static_assert(NI * 1024 == RT * 16 * 64, "items");
#pragma unroll
  for (int k = 0; k < NI; k++) {
    const int i = tid + k * 1024;
    u32* hp = (u32*)(sH + (i >> 6) * HP + (i & 63) * 4);
    const u32 xr = *hp;
    const float x0 = lo16(xr), x1 = hi16(xr);
    const float h0 = x0 * __builtin_amdgcn_rcpf(1.f + __builtin_amdgcn_exp2f(x0 * -1.4426950408889634f));
    const float h1 = x1 * __builtin_amdgcn_rcpf(1.f + __builtin_amdgcn_exp2f(x1 * -1.4426950408889634f));
    *hp = pkbf(h0, h1);
  }
  bar_lds();
  if (wv >= 4) {
    make_b();
    const rsrc_t rp = mk_rsrc(phi, (u32)N * 1024u);
    const u32 oo = (u32)((n0 + 4 * g + (c & 3)) * 1024 + 64 * (wv - 4) + 16 * (c >> 2));
    const bool b0 = c & 1, b1 = c & 2;
#pragma unroll
    for (int rt = 0; rt < RT; rt++) {
      f4 a0 = {0, 0, 0, 0}, a1 = {0, 0, 0, 0};
#pragma unroll
      for (int s = 0; s < 4; s++) {
        u4 A = *(const u4*)(sH + (rt * 16 + c) * HP + (32 * s + 8 * g) * 2);
        a0 = __builtin_amdgcn_mfma_f32_16x16x32_bf16(as_s8(A), as_s8(B[0][s]), a0, 0, 0, 0);
        a1 = __builtin_amdgcn_mfma_f32_16x16x32_bf16(as_s8(A), as_s8(B[1][s]), a1, 0, 0, 0);
      }
      u32 d[4];
#pragma unroll
      for (int r = 0; r < 4; r++) d[r] = pkbf(a0[r], a1[r]);
#pragma unroll
      for (int r = 0; r < 4; r += 2) {
        const u32 t = (u32)__builtin_amdgcn_update_dpp(0, (int)(b0 ? d[r] : d[r + 1]), 0xb1, 0xf, 0xf, true);
        if (b0) d[r] = t; else d[r + 1] = t;
      }
      {
        const u32 t0 = (u32)__builtin_amdgcn_update_dpp(0, (int)(b1 ? d[0] : d[2]), 0x4e, 0xf, 0xf, true);
        const u32 t1 = (u32)__builtin_amdgcn_update_dpp(0, (int)(b1 ? d[1] : d[3]), 0x4e, 0xf, 0xf, true);
        if (b1) { d[0] = t0; d[1] = t1; } else { d[2] = t0; d[3] = t1; }
      }
      u4 o = {d[0], d[1], d[2], d[3]};
      __builtin_amdgcn_raw_buffer_store_b128(o, rp, oo, rt * 16 * 1024, AUX_SC1);
    }
  }
}

template <int NPR, bool VEC>
__global__ __launch_bounds__(1024) void k_prep(const u16* __restrict__ sv, const u16* __restrict__ pos, const int* __restrict__ src,
                                               const int* __restrict__ dst, const u16* __restrict__ w1, const u16* __restrict__ w2,
                                               u16* __restrict__ phi, u4* __restrict__ rec, u4* __restrict__ ovf,
                                               float* __restrict__ split, int N, int E, int P, u32 magicM) {
  extern __shared__ __attribute__((aligned(16))) char lds[];
  if ((int)blockIdx.x < P) filter_role<NPR, VEC>(lds, sv, pos, src, dst, rec, ovf, split, N, E, P, magicM);
  else mlp_role(lds, sv, w1, w2, phi, N, blockIdx.x - P);
}

constexpr int NW = 8;
constexpr int NT = NW * 64;
constexpr int CH = 512;
constexpr int SLOTS = CH + MMAX + 32;
constexpr int KEYN = 24;
constexpr int L_PA = 0;
constexpr int L_PB = L_PA + SLOTS * 16;
constexpr int L_HT = L_PB + SLOTS * 16;
constexpr int L_RAW = L_HT;
constexpr int L_KEY = L_RAW + CH * 16;
constexpr int L_CNT = L_KEY + MMAX * KEYN * 4;
constexpr int L_START = L_CNT + MMAX * 4;
constexpr int L_MISC = L_START + MMAX * 4;
constexpr int L_PEND = L_MISC + 256;
constexpr int L_W = L_PEND + NT * 4;
constexpr int L_TOTAL = L_W + 6 * 32 * 8;
static_assert(L_CNT - L_HT >= 2 * NW * 1024 && L_TOTAL <= 80 * 1024, "LDS layout");

DI f4 mfma444(u2 a, u2 b, f4 c) {
  return __builtin_amdgcn_mfma_f32_4x4x4bf16_1k(__builtin_bit_cast(s4, a), __builtin_bit_cast(s4, b), c, 0, 0, 0);
}

DI void agg_range(char* lds, rsrc_t rphi, rsrc_t rsv, rsrc_t rres, rsrc_t rout, u32 lofs, u32 nb, int lane, int wv, int itb, int ite) {
  const int half = lane >> 5, lp = lane & 31;
  u2 ident;
  ident.x = (lane & 3) == 0 ? 0x00003f80u : ((lane & 3) == 1 ? 0x3f800000u : 0u);
  ident.y = (lane & 3) == 2 ? 0x00003f80u : ((lane & 3) == 3 ? 0x3f800000u : 0u);
  const f4 zero = {0, 0, 0, 0};
  f2 acc[4];
#pragma unroll
  for (int r = 0; r < 4; r++) { f2 z = {0.f, 0.f}; acc[r] = z; }
  const u32 olane = half * 512u + lofs;
  u32 maskw = 0xfffffc00u;
  asm volatile("" : "+s"(maskw));
  const char* pbl = lds + L_PB + half * 16 + itb * 32;
  const char* pal = lds + L_PA + half * 16 + (itb + (lane & 3)) * 32;
  const char* wl = lds + L_W + lp * 8;
  bool pend = false, tail = false;
  if (itb < ite) {
    if (itb > 0) pend = (__builtin_amdgcn_readfirstlane(*(const u32*)(lds + L_PB + (itb - 1) * 32 + 12)) & 1u) == 0;
    tail = (__builtin_amdgcn_readfirstlane(*(const u32*)(lds + L_PB + (ite - 1) * 32 + 12)) & 1u) == 0;
  }
  u32 headso = 0xffffffffu;
  u2 b[4][3]; u32 sw[4], q[4][2];
  auto reduce = [&](f4& o) INL {
#pragma unroll
    for (int r = 0; r < 2; r++) {
      auto x = __builtin_amdgcn_permlane32_swap(__float_as_uint(acc[r].x), __float_as_uint(acc[r + 2].x), false, false);
      auto y = __builtin_amdgcn_permlane32_swap(__float_as_uint(acc[r].y), __float_as_uint(acc[r + 2].y), false, false);
      o[2 * r] = __uint_as_float(x[0]) + __uint_as_float(x[1]);
      o[2 * r + 1] = __uint_as_float(y[0]) + __uint_as_float(y[1]);
    }
  };
  for (int it = itb; it < ite; it += 4, pbl += 128, pal += 128) {
    __builtin_amdgcn_s_setprio(3);
    u32 w[4];
#pragma unroll
    for (int u = 0; u < 4; u++) w[u] = *(const u32*)(pbl + u * 32 + 12);
    const u4 am = *(const u4*)pal;
    u2 wt[3][2];
#pragma unroll
    for (int g = 0; g < 3; g++)
#pragma unroll
      for (int t = 0; t < 2; t++) wt[g][t] = *(const u2*)(wl + (g * 2 + t) * 256);
#pragma unroll
    for (int u = 0; u < 4; u++) {
      sw[u] = __builtin_amdgcn_readfirstlane(w[u]);
      if (u == 0 || it + u < ite) {
        const u32 vo = (w[u] & maskw) | lofs;
        b[u][0].x = bld(rphi, vo, 0);
        b[u][0].y = bld(rphi, vo + 256, 0);
        b[u][1].x = bld(rphi, vo + 512, 0);
        b[u][1].y = bld(rsv, vo + 256, 0);
        b[u][2].x = bld(rsv, vo + 512, 0);
        b[u][2].y = bld(rsv, vo + 768, 0);
        if (sw[u] & 1u) {
          const u32 sq = (nb + ((sw[u] >> 1) & 0x1ffu)) << 10;
          q[u][0] = bld(rres, olane, sq); q[u][1] = bld(rres, olane + 256, sq);
        }
      }
    }
    asm volatile("" ::: "memory");
    __builtin_amdgcn_s_setprio(0);
    f4 wf[3][2];
#pragma unroll
    for (int g = 0; g < 3; g++)
#pragma unroll
      for (int t = 0; t < 2; t++) {
        u2 ah = {am.x, am.y}, al = {am.z, am.w};
        wf[g][t] = mfma444(al, wt[g][t], mfma444(ah, wt[g][t], zero));
      }
    float rr[4][3];
#pragma unroll
    for (int u = 0; u < 4; u++) {
      const u32* rp = (const u32*)(pbl + u * 32);
      rr[u][0] = __uint_as_float(rp[0]); rr[u][1] = __uint_as_float(rp[1]); rr[u][2] = __uint_as_float(rp[2]);
    }
#pragma unroll
    for (int u = 0; u < 4; u++) {
      if (u == 0 || it + u < ite) {
        const f4 U0 = mfma444(ident, b[u][0], zero);
        const f4 U1 = mfma444(ident, b[u][1], zero);
        const f4 U2 = mfma444(ident, b[u][2], zero);
        acc[0].x = __builtin_fmaf(U0[0], wf[0][0][u], acc[0].x);
        acc[0].y = __builtin_fmaf(U0[1], wf[0][1][u], acc[0].y);
        const float mv0 = U0[2] * wf[1][0][u], mv1 = U0[3] * wf[1][1][u];
        const float mr0 = U1[0] * wf[2][0][u], mr1 = U1[1] * wf[2][1][u];
        acc[1].x = __builtin_fmaf(mv0, U1[2], acc[1].x); acc[1].y = __builtin_fmaf(mv1, U1[3], acc[1].y);
        acc[2].x = __builtin_fmaf(mv0, U2[0], acc[2].x); acc[2].y = __builtin_fmaf(mv1, U2[1], acc[2].y);
        acc[3].x = __builtin_fmaf(mv0, U2[2], acc[3].x); acc[3].y = __builtin_fmaf(mv1, U2[3], acc[3].y);
        acc[1].x = __builtin_fmaf(mr0, rr[u][0], acc[1].x); acc[1].y = __builtin_fmaf(mr1, rr[u][0], acc[1].y);
        acc[2].x = __builtin_fmaf(mr0, rr[u][1], acc[2].x); acc[2].y = __builtin_fmaf(mr1, rr[u][1], acc[2].y);
        acc[3].x = __builtin_fmaf(mr0, rr[u][2], acc[3].x); acc[3].y = __builtin_fmaf(mr1, rr[u][2], acc[3].y);
        if (sw[u] & 1u) {
          const u32 so = (nb + ((sw[u] >> 1) & 0x1ffu)) << 10;
          f4 o;
          reduce(o);
          u2 qq = {q[u][0], q[u][1]};
          o = mfma444(ident, qq, o);
          if (pend) {
            *(f4*)(lds + L_HT + (wv * 64 + lane) * 16) = o;
            headso = so; pend = false;
          } else {
            bst(pkbf(o[0], o[1]), rout, olane, so);
            bst(pkbf(o[2], o[3]), rout, olane + 256, so);
          }
#pragma unroll
          for (int r = 0; r < 4; r++) { f2 z = {0.f, 0.f}; acc[r] = z; }
        }
      }
    }
  }
  char* hl = lds + L_HT + (wv * 64 + lane) * 16;
  if (tail) {
    f4 o;
    reduce(o);
    *(f4*)(hl + NW * 1024) = o;
  }
  if (lane == 0) *(u32*)(lds + L_MISC + (32 + wv) * 4) = (tail ? 1u : 0u) | ((pend || itb >= ite) ? 2u : 0u);
  bar_lds();
  if (headso != 0xffffffffu) {
    f4 o = *(const f4*)hl;
    for (int k = wv - 1; k >= 0; k--) {
      const u32 st = __builtin_amdgcn_readfirstlane(*(const u32*)(lds + L_MISC + (32 + k) * 4));
      if (st & 1u) o += *(const f4*)(lds + L_HT + ((NW + k) * 64 + lane) * 16);
      if (!(st & 2u)) break;
    }
    bst(pkbf(o[0], o[1]), rout, olane, headso);
    bst(pkbf(o[2], o[3]), rout, olane + 256, headso);
  }
}

NOPK __global__ __launch_bounds__(NT, 4) void k_agg(const u16* __restrict__ sv, const u16* __restrict__ pos, const u16* __restrict__ freqs,
                                                    const u16* __restrict__ wr, const u16* __restrict__ phi, const u4* __restrict__ rec,
                                                    const u4* __restrict__ ovf, const float* __restrict__ split, u16* out, int N, int P, int M) {
  __shared__ __attribute__((aligned(16))) char lds[L_TOTAL];
  const int tid = threadIdx.x, lane = tid & 63, wv = __builtin_amdgcn_readfirstlane(tid >> 6);
  const int label = blockIdx.x & 7, q = label >> 1, h = label & 1, j = blockIdx.x >> 3;
  const int cell = q * JG + j, nb = j * M;
  u32* cnt = (u32*)(lds + L_CNT);
  u32* start = (u32*)(lds + L_START);
  u32* misc = (u32*)(lds + L_MISC);
  u4* raw = (u4*)(lds + L_RAW);
  u4* pa = (u4*)(lds + L_PA);
  u4* pb = (u4*)(lds + L_PB);
  const char* svb = (const char*)sv;
  constexpr int RND = (PMAX * SLOTK + NT - 1) / NT;

  const u32 nc = min(nb + tid, N - 1);
  const u16* pp = (const u16*)((const char*)pos + nc * 6u);
  const u32 px = pp[0], py = pp[1];
  const float sx = split[0], sy = split[1];
  __builtin_amdgcn_sched_barrier(0);
  u4 pre[RND];
#pragma unroll
  for (int i = 0; i < RND; i++) pre[i] = rec[(u32)cell * (u32)(P * SLOTK) + min(tid + i * NT, P * SLOTK - 1)];
  constexpr int NWE = 6 * 32;
  u32 wk[4];
  {
    const int we = min(tid, NWE - 1), gt = we / 32, l = we % 32;
    const u16* wp = wr + (gt >> 1) * 128 + h * 64 + 2 * l + (gt & 1);
#pragma unroll
    for (int k = 0; k < 4; k++) wk[k] = wp[k * 384];
  }
  const u32 fq01 = *(const u32*)freqs, fq23 = *(const u32*)(freqs + 2);
  __builtin_amdgcn_sched_barrier(0);
  for (int i = tid; i < MMAX; i += NT) cnt[i] = 0;
  for (int i = tid; i < MMAX * KEYN / 4; i += NT) { u4 f = {~0u, ~0u, ~0u, ~0u}; *(u4*)(lds + L_KEY + i * 16) = f; }
  if (tid < 2) misc[tid] = 0;
  bar_lds();

  const bool member = tid < M && nb + tid < N && quadrant(lo16(px), lo16(py), sx, sy) == (u32)q;
  auto stage = [&](u4 t, u32 pos_) INL {
    const u32 rnk = atomicAdd(&cnt[t.y - nb], 1u);
    t.w = (t.w & 0xffffu) | (rnk << 16);
    raw[pos_] = t;
    if (rnk < KEYN) *(u32*)(lds + L_KEY + ((t.y - nb) * KEYN + rnk) * 4) = (t.x << 8) | rnk;
  };
  {
    u32 pendm = 0;
    unsigned long long vm[RND], vm2[RND];
    u4 pre2[RND];
    bool ovfl = false;
#pragma unroll
    for (int i = 0; i < RND; i++) {
      const int sl = tid + i * NT;
      const u32 c = sl < P * SLOTK ? pre[i].w >> 16 : 0u, k = sl % SLOTK;
      const bool v2 = k + SLOTK < c;
      vm[i] = ballot64(k < c);
      vm2[i] = ballot64(v2);
      if (vm2[i]) pre2[i] = ovf[v2 ? (u32)(sl / SLOTK) * CHUNK + (pre[i].y >> 16) + k : 0u];
      pre[i].y &= 0xffffu;
      ovfl |= c > 2 * SLOTK;
    }
    u32 tot = 0;
#pragma unroll
    for (int i = 0; i < RND; i++) tot += __popcll(vm[i]) + __popcll(vm2[i]);
    u32 base = 0;
    if (lane == 0) base = atomicAdd(&misc[0], tot);
    if (ballot64(ovfl) != 0 && lane == 0) misc[1] = 1;
    base = __builtin_amdgcn_readfirstlane(base);
#pragma unroll
    for (int i = 0; i < RND; i++) {
      const u32 pos_ = base + mbcnt64(vm[i]);
      base += __popcll(vm[i]);
      if (vm[i] >> lane & 1) { if (pos_ < CH) stage(pre[i], pos_); else pendm |= 1u << i; }
    }
#pragma unroll
    for (int i = 0; i < RND; i++) {
      if (vm2[i]) {
        const u32 pos_ = base + mbcnt64(vm2[i]);
        base += __popcll(vm2[i]);
        if (vm2[i] >> lane & 1) { if (pos_ < CH) stage(pre2[i], pos_); else pendm |= 1u << (RND + i); }
      }
    }
    *(u32*)(lds + L_PEND + tid * 4) = pendm;
  }
  if (tid < NWE) { u2 t = {wk[0] | (wk[1] << 16), wk[2] | (wk[3] << 16)}; *(u2*)(lds + L_W + tid * 8) = t; }
  const rsrc_t rphi = mk_rsrc(phi, (u32)N * 1024u), rout = mk_rsrc(out, (u32)N * 1024u);
  bar_lds();
  u32 touch[4];
#pragma unroll
  for (int i = 0; i < 4; i++) touch[i] = *(const u32*)(svb + (member ? nc * 1024u + i * 256u + h * 128u : 0u));
  const u32 nslot = __builtin_amdgcn_readfirstlane(misc[0]);
  const bool anyovf = __builtin_amdgcn_readfirstlane(misc[1]) != 0;
  u32 total = nslot;
  auto stage_ovf = [&](u32 c0, int lane_) INL -> u32 {
    u32 pos_ = nslot;
    for (int p0 = 0; p0 < P; p0 += 64) {
      u4 r0 = {0, 0, 0, 0};
      if (p0 + lane_ < P) r0 = rec[(u32)(cell * P + p0 + lane_) * SLOTK];
      const u32 d = r0.y >> 16, c = r0.w >> 16, n = c > 2 * SLOTK ? c - 2 * SLOTK : 0u;
      const u32 inc = wave_scan(n);
      if (wv == 0) {
        const u32 my = pos_ + inc - n;
        for (u32 k = 0; k < n; k++)
          if (my + k >= c0 && my + k < c0 + CH) stage(ovf[(size_t)(p0 + lane_) * CHUNK + d + SLOTK + k], my + k - c0);
      }
      pos_ += __builtin_amdgcn_readlane(inc, 63);
    }
    return pos_;
  };
  if (anyovf) { total = stage_ovf(0u, lane); bar_lds(); }

  auto process = [&](const bool FIRST, u32 nrec, int tid_) INL {
    const int lane = tid_ & 63;
    const u32 lofs = h * 128 + (lane & 31) * 4;
    int T;
    {
      const u4 c4 = *(const u4*)(cnt + lane * MPL);
      u32 it[MPL] = {c4.x, c4.y, c4.z, c4.w}, sum = 0;
#pragma unroll
      for (int i = 0; i < MPL; i++) { it[i] = (it[i] + 1) >> 1; sum += it[i]; }
      const u32 inc = wave_scan(sum);
      T = (int)__builtin_amdgcn_readlane(inc, 63);
      u32 ex = inc - sum;
      u4 s4;
#pragma unroll
      for (int k = 0; k < MPL; k++) { s4[k] = ex; ex += it[k]; }
      *(u4*)(start + lane * MPL) = s4;
    }
    const float frq[4] = {lo16(fq01), hi16(fq01), lo16(fq23), hi16(fq23)};
    for (u32 i0 = wv * 64; i0 < nrec; i0 += NT) {
      const u32 i = i0 + lane;
      const u4 t = raw[min(i, (u32)CH - 1)];
      const u32 loc = min(t.y - nb, (u32)MMAX - 1);
      u32 rnk = t.w >> 16;
      {
        const u32 cc = cnt[loc], mine = (t.x << 8) | rnk;
        const u4* kp = (const u4*)(lds + L_KEY + loc * (KEYN * 4));
        u32 rk = 0;
#pragma unroll
        for (int b = 0; b < KEYN / 8; b++) {
          if (b == 0 || ballot64(cc > 8 * b) != 0) {
            const u4 k0 = kp[2 * b], k1 = kp[2 * b + 1];
#pragma unroll
            for (int k = 0; k < 4; k++) rk += (k0[k] < mine ? 1u : 0u) + (k1[k] < mine ? 1u : 0u);
          }
        }
        if (cc <= KEYN) rnk = rk;
      }
      const float rx = lo16(t.z), ry = hi16(t.z), rz = lo16(t.w);
      const u32 s01 = pkbf(rx * rx, ry * ry);
      float ss = lo16(s01) + hi16(s01) + rbf(rz * rz);
      ss = rbf(ss); ss = rbf(ss + 1e-8f);
      const float dd = rbf(__builtin_amdgcn_sqrtf(ss));
      const float id = __builtin_amdgcn_rcpf(dd);
      const u32 rh01 = pkbf(rx * id, ry * id);
      u4 ca, cb;
      cb.x = rh01 << 16; cb.y = rh01 & 0xffff0000u; cb.z = pkbf(0.f, rz * id) & 0xffff0000u;
      float fc = rbf(3.14159265358979f * dd);
      fc = rbf(fc * 0.2f);
      fc = rbf(__builtin_amdgcn_cosf(__builtin_amdgcn_fractf(fc * 0.15915494309189535f)));
      fc = 0.5f * rbf(fc + 1.0f);
      if (!(dd < 5.0f)) fc = 0.0f * dd;
      float a[4];
#pragma unroll
      for (int k = 0; k < 4; k += 2) {
        const u32 x = pkbf(dd * frq[k], dd * frq[k + 1]);
        const u32 y = pkbf(__builtin_amdgcn_sinf(__builtin_amdgcn_fractf(lo16(x) * 0.15915494309189535f)),
                           __builtin_amdgcn_sinf(__builtin_amdgcn_fractf(hi16(x) * 0.15915494309189535f)));
        const u32 z = pkbf(lo16(y) * id, hi16(y) * id);
        a[k] = lo16(z) * fc; a[k + 1] = hi16(z) * fc;
      }
      ca.x = pkbf(a[0], a[1]); ca.y = pkbf(a[2], a[3]);
      ca.z = pkbf(a[0] - lo16(ca.x), a[1] - hi16(ca.x)); ca.w = pkbf(a[2] - lo16(ca.y), a[3] - hi16(ca.y));
      if (i < nrec) {
        const u32 st = start[loc], cc = cnt[loc], sl = 2 * st + rnk;
        const u32 last = (rnk | 1u) + 1 >= cc;
        cb.w = (t.x << 10) | (loc << 1) | last;
        pa[sl] = ca; pb[sl] = cb;
        if (rnk + 1 == cc && (cc & 1)) { u4 z = {0, 0, 0, 0}; pa[sl + 1] = z; u4 t2 = {0, 0, 0, cb.w}; pb[sl + 1] = t2; }
      }
    }
    if (tid_ < 32) { u4 z = {0, 0, 0, 0}; pa[2 * T + tid_] = z; pb[2 * T + tid_] = z; }
    bar_lds();
    const int itb = (wv * T) / NW, ite = ((wv + 1) * T) / NW;
#pragma unroll
    for (int i = 0; i < 4; i++) asm volatile("" ::"v"(touch[i]));
    agg_range(lds, rphi, mk_rsrc(sv, (u32)N * 1024u), mk_rsrc(FIRST ? sv : (const u16*)out, (u32)N * 1024u), rout, lofs, nb, lane, wv, itb, ite);
  };
  unsigned long long em = ballot64(member && cnt[min(tid, MMAX - 1)] == 0);
  u32 left = nslot > CH ? nslot - CH : 0u;
  u32 c0 = max(nslot, (u32)CH);
  u32 nrec = min(total, (u32)CH);
  bool first = true;
  for (;;) {
    int tz = threadIdx.x;
    asm volatile("" : "+v"(tz));
    process(first, nrec, tz);
    asm volatile("" : "+v"(tz));
    const int lz = tz & 63;
    if (em) {
      const u32 ol = (lz >> 5) * 512u + h * 128 + (lz & 31) * 4;
      const rsrc_t rs = mk_rsrc(sv, (u32)N * 1024u);
      while (em) {
        u32 so[4], v[4][2];
#pragma unroll
        for (int k = 0; k < 4; k++) {
          so[k] = em ? (u32)(nb + wv * 64 + __builtin_ctzll(em)) << 10 : OOB;
          em &= em - 1;
          v[k][0] = bld(rs, ol, so[k]); v[k][1] = bld(rs, ol + 256, so[k]);
        }
#pragma unroll
        for (int k = 0; k < 4; k++) { bst(v[k][0], rout, ol, so[k]); bst(v[k][1], rout, ol + 256, so[k]); }
      }
    }
    if (left == 0 && c0 >= total) break;
    first = false;
    bar_all();
    for (int i = tz; i < MMAX; i += NT) cnt[i] = 0;
    for (int i = tz; i < MMAX * KEYN / 4; i += NT) { u4 f = {~0u, ~0u, ~0u, ~0u}; *(u4*)(lds + L_KEY + i * 16) = f; }
    if (tz == 0) misc[0] = 0;
    bar_lds();
    if (left) {
      u32 pendm = *(const u32*)(lds + L_PEND + tz * 4);
#pragma unroll
      for (int i = 0; i < 2 * RND; i++) {
        const unsigned long long m = ballot64(pendm >> i & 1);
        if (m) {
          u32 base = 0;
          if (lz == 0) base = atomicAdd(&misc[0], (u32)__popcll(m));
          base = __builtin_amdgcn_readfirstlane(base);
          const u32 pos_ = base + mbcnt64(m);
          if ((pendm >> i & 1) && pos_ < CH) {
            const int sl = tz + (i % RND) * NT;
            u4 r0 = rec[(u32)cell * (u32)(P * SLOTK) + sl];
            if (i < RND) { r0.y &= 0xffffu; stage(r0, pos_); }
            else stage(ovf[(u32)(sl / SLOTK) * CHUNK + (r0.y >> 16) + sl % SLOTK], pos_);
            pendm &= ~(1u << i);
          }
        }
      }
      *(u32*)(lds + L_PEND + tz * 4) = pendm;
      nrec = min(left, (u32)CH);
      left -= nrec;
    } else {
      stage_ovf(c0, lz);
      nrec = min(total - c0, (u32)CH);
      c0 += CH;
    }
    bar_lds();
  }
}

__global__ void fb_mlp(const u16* sv, const u16* w1, const u16* w2, u16* phi) {
  __shared__ float s[128], hh[128];
  int n = blockIdx.x, t = threadIdx.x;
  s[t] = lo16(sv[(size_t)n * 512 + t]);
  __syncthreads();
  float acc = 0.f;
  for (int k = 0; k < 128; k++) acc += s[k] * lo16(w1[k * 128 + t]);
  acc = rbf(acc);
  hh[t] = rbf(acc / (1.f + expf(-acc)));
  __syncthreads();
  for (int g = 0; g < 3; g++) {
    float a = 0.f;
    for (int k = 0; k < 128; k++) a += hh[k] * lo16(w2[k * 384 + g * 128 + t]);
    phi[(size_t)n * 384 + g * 128 + t] = (u16)pkbf(a, 0.f);
  }
}
__global__ void fb_edge(const u16* sv, const u16* pos, const int* src, const int* dst, const u16* freqs, const u16* wr,
                        const u16* phi, float* acc) {
  int e = blockIdx.x, t = threadIdx.x;
  size_t s = src[e], d = dst[e];
  float r[3], ss = 0.f;
  for (int x = 0; x < 3; x++) { r[x] = rbf(lo16(pos[s * 3 + x]) - lo16(pos[d * 3 + x])); ss += rbf(r[x] * r[x]); }
  ss = rbf(ss); ss = rbf(ss + 1e-8f);
  float dd = rbf(sqrtf(ss));
  if (dd >= 5.0f && dd <= 3.0e38f) return;
  float fc = rbf(3.14159265358979f * dd); fc = rbf(fc / 5.0f); fc = rbf(cosf(fc)); fc = rbf(fc + 1.0f); fc = rbf(0.5f * fc);
  if (!(dd < 5.0f)) fc = 0.0f * dd;
  float wf[3] = {0.f, 0.f, 0.f};
  for (int k = 0; k < 4; k++) {
    float a = rbf(rbf(sinf(rbf(dd * lo16(freqs[k])))) / dd) * fc;
    for (int g = 0; g < 3; g++) wf[g] += a * lo16(wr[k * 384 + g * 128 + t]);
  }
  atomicAdd(&acc[d * 512 + t], lo16(phi[s * 384 + t]) * wf[0]);
  float mv = lo16(phi[s * 384 + 128 + t]) * wf[1], mr = lo16(phi[s * 384 + 256 + t]) * wf[2];
  for (int x = 0; x < 3; x++) atomicAdd(&acc[d * 512 + 128 + x * 128 + t], mv * lo16(sv[s * 512 + 128 + x * 128 + t]) + mr * rbf(r[x] / dd));
}
__global__ void fb_fin(const u16* sv, const float* acc, u16* out, size_t n) {
  size_t i = blockIdx.x * (size_t)256 + threadIdx.x;
  if (i < n) out[i] = (u16)pkbf(lo16(sv[i]) + acc[i], 0.f);
}

extern "C" void kernel_launch(void* const* d_in, const int* in_sizes, int n_in, void* d_out, int out_size, void* d_ws,
                              size_t ws_size, hipStream_t stream) {
  const u16* sv = (const u16*)d_in[0];
  const u16* pos = (const u16*)d_in[1];
  const int* src = (const int*)d_in[2];
  const int* dst = (const int*)d_in[3];
  const u16* freqs = (const u16*)d_in[4];
  const u16* w1 = (const u16*)d_in[5];
  const u16* w2 = (const u16*)d_in[6];
  const u16* wr = (const u16*)d_in[7];
  u16* out = (u16*)d_out;
  const int N = in_sizes[0] / 512, E = in_sizes[2];
  const int P = (E + CHUNK - 1) / CHUNK;
  const int M = (N + JG - 1) / JG;
  char* ws = (char*)d_ws;
  u16* phi = (u16*)ws;
  size_t off = ((size_t)N * 1024 + 255) & ~(size_t)255;
  if (M >= 2 && N <= 15000 && E >= 4 && M <= MMAX && P <= PMAX) {
    u4* rec = (u4*)(ws + off); off += (size_t)NCELL * P * SLOTK * 16;
    u4* ovf = (u4*)(ws + off); off += (size_t)P * CHUNK * 16;
    float* split = (float*)(ws + off);
    const u32 magicM = (u32)(0x100000000ull / (u32)M) + 1u;
    const int nmlp = (N + RT * 16 - 1) / (RT * 16);
    size_t lds1 = (size_t)((N + 1) & ~1) * 8 + NCELL * 8 + 256 + NCELL * SLOTK * 16;
    const size_t ldsm = 2 * RT * 16 * HP;
    if (lds1 < ldsm) lds1 = ldsm;
#define LAUNCH_PREP(NPR_, VEC_) do { \
      (void)hipFuncSetAttribute((const void*)k_prep<NPR_, VEC_>, hipFuncAttributeMaxDynamicSharedMemorySize, 160 * 1024); \
      k_prep<NPR_, VEC_><<<P + nmlp, 1024, lds1, stream>>>(sv, pos, src, dst, w1, w2, phi, rec, ovf, split, N, E, P, magicM); } while (0)
    if (N <= 7 * 2048 && (E & 3) == 0) LAUNCH_PREP(7, true);
    else if ((E & 3) == 0) LAUNCH_PREP(8, true);
    else LAUNCH_PREP(8, false);
    k_agg<<<8 * JG, NT, 0, stream>>>(sv, pos, freqs, wr, phi, rec, ovf, split, out, N, P, M);
  } else {
    float* acc = (float*)(ws + off);
    (void)hipMemsetAsync(acc, 0, (size_t)N * 512 * 4, stream);
    fb_mlp<<<N, 128, 0, stream>>>(sv, w1, w2, phi);
    if (E > 0) fb_edge<<<E, 128, 0, stream>>>(sv, pos, src, dst, freqs, wr, phi, acc);
    fb_fin<<<(unsigned)(((size_t)N * 512 + 255) / 256), 256, 0, stream>>>(sv, acc, out, (size_t)N * 512);
  }
}
```

```cpp
#include <hip/hip_runtime.h>
#include <stdint.h>

typedef unsigned short u16;
typedef unsigned int u32;
typedef float f2 __attribute__((ext_vector_type(2)));
typedef float f4 __attribute__((ext_vector_type(4)));
typedef u32 u2 __attribute__((ext_vector_type(2)));
typedef u32 u4 __attribute__((ext_vector_type(4)));
typedef short s4 __attribute__((ext_vector_type(4)));
typedef short s8 __attribute__((ext_vector_type(8)));
typedef __bf16 bf2 __attribute__((ext_vector_type(2)));

#define DI __device__ __forceinline__
#define INL __attribute__((always_inline))
#if defined(__HIP_DEVICE_COMPILE__)
#define NOPK __attribute__((target("no-packed-fp32-ops")))
#else
#define NOPK
#endif

constexpr int NQ = 4;
constexpr int JG = 64;
constexpr int NCELL = NQ * JG;
constexpr int CHUNK = 4096;
constexpr int RT = 5;
constexpr int MPL = 4;
constexpr int MMAX = MPL * 64;
constexpr int PMAX = 128;
constexpr int SLOTK = 8;
constexpr int AUX_NT = 2, AUX_SC1 = 16;
constexpr u32 OOB = 0x40000000u;

DI float lo16(u32 x) { return __uint_as_float(x << 16); }
DI float hi16(u32 x) { return __uint_as_float(x & 0xffff0000u); }
DI u32 pkbf(float a, float b) { f2 f = {a, b}; bf2 v = __builtin_convertvector(f, bf2); return __builtin_bit_cast(u32, v); }
DI float rbf(float a) { return lo16(pkbf(a, 0.f)); }

DI void bar_lds() {
  asm volatile("s_waitcnt lgkmcnt(0)" ::: "memory");
  __builtin_amdgcn_s_barrier();
  asm volatile("" ::: "memory");
}
DI void bar_all() {
  asm volatile("s_waitcnt vmcnt(0) lgkmcnt(0)" ::: "memory");
  __builtin_amdgcn_s_barrier();
  asm volatile("" ::: "memory");
}
DI unsigned long long ballot64(bool p) { return __builtin_amdgcn_ballot_w64(p); }
DI u32 mbcnt64(unsigned long long m) { return __builtin_amdgcn_mbcnt_hi((u32)(m >> 32), __builtin_amdgcn_mbcnt_lo((u32)m, 0)); }
typedef __amdgpu_buffer_rsrc_t rsrc_t;
DI rsrc_t mk_rsrc(const void* p, u32 bytes) { return __builtin_amdgcn_make_buffer_rsrc((void*)p, 0, bytes, 0x00020000); }
DI u32 bld(rsrc_t r, u32 vo, u32 so) { return __builtin_amdgcn_raw_buffer_load_b32(r, vo, so, 0); }
DI void bst(u32 v, rsrc_t r, u32 vo, u32 so) { __builtin_amdgcn_raw_buffer_store_b32(v, r, vo, so, AUX_NT); }

template <int CTRL, int RMASK> DI u32 dpp_mov(u32 x) { return (u32)__builtin_amdgcn_update_dpp(0, (int)x, CTRL, RMASK, 0xf, false); }
DI u32 wave_scan(u32 x) {
  x += dpp_mov<0x111, 0xf>(x);
  x += dpp_mov<0x112, 0xf>(x);
  x += dpp_mov<0x114, 0xf>(x);
  x += dpp_mov<0x118, 0xf>(x);
  x += dpp_mov<0x142, 0xa>(x);
  x += dpp_mov<0x143, 0xc>(x);
  return x;
}
template <int CTRL, int RMASK> DI float dpp_addf(float x) { return x + __uint_as_float(dpp_mov<CTRL, RMASK>(__float_as_uint(x))); }
DI float row_scanf(float x) {
  x = dpp_addf<0x111, 0xf>(x); x = dpp_addf<0x112, 0xf>(x); x = dpp_addf<0x114, 0xf>(x); x = dpp_addf<0x118, 0xf>(x);
  return x;
}
DI float wave_scanf(float x) {
  x = row_scanf(x); x = dpp_addf<0x142, 0xa>(x); x = dpp_addf<0x143, 0xc>(x);
  return x;
}
DI float rdlanef(float x, int l) { return __uint_as_float(__builtin_amdgcn_readlane(__float_as_uint(x), l)); }
DI u32 quadrant(float x, float y, float sx, float sy) { return (x > sx ? 1u : 0u) | (y > sy ? 2u : 0u); }

template <int NPR, bool VEC>
DI void filter_role(char* lds, const u16* __restrict__ sv, const u16* __restrict__ pos, const int* __restrict__ src, const int* __restrict__ dst,
                    u4* __restrict__ rec, u4* __restrict__ ovf, float* __restrict__ split, int N, int E, int P, u32 magicM) {
  const int tid = threadIdx.x, lane = tid & 63, wv = __builtin_amdgcn_readfirstlane(tid >> 6), p = blockIdx.x;
  u32* hist = (u32*)lds;
  u32* ooff = hist + NCELL;
  f4* red = (f4*)(lds + NCELL * 8);
  u4* img = (u4*)(lds + NCELL * 8 + 256);
  u2* tab = (u2*)(lds + NCELL * 8 + 256 + NCELL * SLOTK * 16);
  const int e0 = p * CHUNK + tid * 4;
  const int npair = N >> 1;
  const char* posb = (const char*)pos;
  u32 sm0, sm1, sm2;
  { const u32* t = (const u32*)(posb + (u32)(12 * (int)(((unsigned)tid * (unsigned)npair) >> 10))); sm0 = t[0]; sm1 = t[1]; sm2 = t[2]; }
  u32 pw[NPR][3];
#pragma unroll
  for (int r = 0; r < NPR; r++) {
    const u32* t = (const u32*)(posb + (u32)(12 * min(tid + r * 1024, npair - 1)));
    pw[r][0] = t[0]; pw[r][1] = t[1]; pw[r][2] = t[2];
  }
  int sI[4], dI[4];
  if (VEC) {
    const int ec = min(e0, E - 4);
    int4 a = *(const int4*)(src + ec), b = *(const int4*)(dst + ec);
    sI[0] = a.x; sI[1] = a.y; sI[2] = a.z; sI[3] = a.w;
    dI[0] = b.x; dI[1] = b.y; dI[2] = b.z; dI[3] = b.w;
  } else {
#pragma unroll
    for (int u = 0; u < 4; u++) { const int ec = min(e0 + u, E - 1); sI[u] = src[ec]; dI[u] = dst[ec]; }
  }
  if (tid < NCELL) hist[tid] = 0;
  {
    const float sx = wave_scanf(lo16(sm0) + hi16(sm1)), sy = wave_scanf(hi16(sm0) + lo16(sm2));
    if (lane == 63) { f4 t = {sx, sy, 0.f, 0.f}; red[wv] = t; }
  }
#pragma unroll
  for (int r = 0; r < NPR; r++) {
    const int i = tid + r * 1024;
    if (i < npair) {
      u4 t;
      t.x = pw[r][0]; t.y = pw[r][1] & 0xffffu;
      t.z = __builtin_amdgcn_alignbit(pw[r][2], pw[r][1], 16); t.w = pw[r][2] >> 16;
      *(u4*)(tab + 2 * i) = t;
    }
  }
  if ((N & 1) && tid == 0) {
    const u32 n0 = N - 1;
    u2 t = {(u32)pos[3 * n0] | ((u32)pos[3 * n0 + 1] << 16), (u32)pos[3 * n0 + 2]};
    tab[n0] = t;
  }
  bar_lds();
  float sx, sy;
  {
    const f4 t = red[lane & 15];
    sx = rdlanef(row_scanf(t.x), 15) * (1.f / 2048.f); sy = rdlanef(row_scanf(t.y), 15) * (1.f / 2048.f);
  }
  if (p == 0 && tid == 0) { split[0] = sx; split[1] = sy; }
  u32 rk[4], rxy[4], rz[4];
  u2 ta[4], tb[4];
#pragma unroll
  for (int u = 0; u < 4; u++) { ta[u] = tab[sI[u]]; tb[u] = tab[dI[u]]; }
#pragma unroll
  for (int u = 0; u < 4; u++) {
    const u2 a = ta[u], b = tb[u];
    const float bx = lo16(b.x), by = hi16(b.x);
    const float rx = lo16(a.x) - bx, ry = hi16(a.x) - by, rzz = lo16(a.y) - lo16(b.y);
    const float d2 = rx * rx + ry * ry + rzz * rzz;
    const bool cand = (e0 + u < E) && !(d2 >= 25.25f && d2 <= 3.0e38f);
    rk[u] = 0xffffffffu;
    if (cand) {
      const u32 cell = quadrant(bx, by, sx, sy) * JG + __umulhi((u32)dI[u], magicM);
      rk[u] = atomicAdd(&hist[cell], 1u) | (cell << 16);
    }
    rxy[u] = pkbf(rx, ry); rz[u] = pkbf(rzz, 0.f);
  }
  bar_lds();
  if (wv == 0) {
    constexpr int CPL = NCELL / 64;
    u32 c[CPL], s = 0;
#pragma unroll
    for (int i = 0; i < CPL; i++) c[i] = hist[lane * CPL + i];
#pragma unroll
    for (int i = 0; i < CPL; i++) s += c[i] > SLOTK ? c[i] - SLOTK : 0u;
    u32 ex = wave_scan(s) - s;
#pragma unroll
    for (int i = 0; i < CPL; i++) { ooff[lane * CPL + i] = ex; ex += c[i] > SLOTK ? c[i] - SLOTK : 0u; }
  }
  bool anyo = false;
#pragma unroll
  for (int u = 0; u < 4; u++) {
    if (rk[u] != 0xffffffffu && (rk[u] & 0xffffu) < SLOTK) {
      u4 t = {(u32)sI[u], (u32)dI[u], rxy[u], (rz[u] & 0xffffu) | (hist[rk[u] >> 16] << 16)};
      img[(rk[u] >> 16) * SLOTK + (rk[u] & 0xffffu)] = t;
    }
    anyo |= rk[u] != 0xffffffffu && (rk[u] & 0xffffu) >= SLOTK;
  }
  for (int i = tid; i < NCELL * SLOTK; i += 1024) {
    const u32 c = hist[i / SLOTK];
    if ((u32)(i % SLOTK) >= c) { u4 t = {0, 0, 0, c << 16}; img[i] = t; }
  }
  bar_lds();
  {
    const rsrc_t rr = mk_rsrc(rec, (u32)(NCELL * P * SLOTK) * 16u);
    for (int i = tid; i < NCELL * SLOTK; i += 1024) {
      u4 t = img[i];
      if ((t.w >> 16) > SLOTK) t.y |= ooff[i / SLOTK] << 16;
      __builtin_amdgcn_raw_buffer_store_b128(t, rr, (u32)((i / SLOTK) * P + p) * (SLOTK * 16u) + (i % SLOTK) * 16u, 0, 0);
    }
  }
  if (anyo) {
#pragma unroll
    for (int u = 0; u < 4; u++) {
      if (rk[u] != 0xffffffffu && (rk[u] & 0xffffu) >= SLOTK) {
        const u32 idx = ooff[rk[u] >> 16] + (rk[u] & 0xffffu) - SLOTK;
        u4 t = {(u32)sI[u], (u32)dI[u], rxy[u], rz[u]};
        ovf[(size_t)p * CHUNK + idx] = t;
      }
    }
  }
}

constexpr int HP = 272;
DI s8 as_s8(u4 x) { return __builtin_bit_cast(s8, x); }

DI void mlp_role(char* lds, const u16* __restrict__ sv, const u16* __restrict__ w1, const u16* __restrict__ w2,
                 u16* __restrict__ phi, int N, int tile) {
  const int tid = threadIdx.x, lane = tid & 63, wv = __builtin_amdgcn_readfirstlane(tid >> 6);
  const int g = lane >> 4, c = lane & 15;
  const int n0 = tile * (RT * 16);
  char* sS = lds;
  char* sH = lds + RT * 16 * HP;
  const char* svb = (const char*)sv;
  u4 sreg[2];
#pragma unroll
  for (int r = 0; r < 2; r++) {
    const int i = min(tid + r * 1024, RT * 256 - 1), row = i >> 4, ch = i & 15;
    sreg[r] = *(const u4*)(svb + (u32)(min(n0 + row, N - 1) * 1024 + ch * 16));
  }
  const char* wp = (const char*)((wv < 4) ? (w1 + 32 * wv) : (w2 + 32 * (wv - 4)));
  const int wld = (wv < 4) ? 256 : 768;
  u32 L[32];
  if (wv >= 4) __builtin_amdgcn_s_sleep(6);
  {
    const u32 wo = (u32)(8 * g * wld + 4 * c);
#pragma unroll
    for (int s = 0; s < 4; s++)
#pragma unroll
      for (int j = 0; j < 8; j++) L[s * 8 + j] = *(const u32*)(wp + (size_t)((32 * s + j) * wld) + wo);
  }
#pragma unroll
  for (int r = 0; r < 2; r++) {
    const int i = tid + r * 1024, row = i >> 4, ch = i & 15;
    if (row < RT * 16) *(u4*)(sS + row * HP + ch * 16) = sreg[r];
  }
  bar_lds();
  u4 B[2][4];
  auto make_b = [&]() INL {
#pragma unroll
    for (int i = 0; i < 32; i += 8)
      asm volatile("" : "+v"(L[i]), "+v"(L[i + 1]), "+v"(L[i + 2]), "+v"(L[i + 3]), "+v"(L[i + 4]), "+v"(L[i + 5]), "+v"(L[i + 6]), "+v"(L[i + 7]));
#pragma unroll
    for (int s = 0; s < 4; s++)
#pragma unroll
      for (int i = 0; i < 4; i++) {
        B[0][s][i] = __builtin_amdgcn_perm(L[s * 8 + 2 * i + 1], L[s * 8 + 2 * i], 0x05040100u);
        B[1][s][i] = __builtin_amdgcn_perm(L[s * 8 + 2 * i + 1], L[s * 8 + 2 * i], 0x07060302u);
      }
  };
  if (wv < 4) {
    make_b();
    f4 a0[RT], a1[RT];
#pragma unroll
    for (int rt = 0; rt < RT; rt++) { f4 z = {0, 0, 0, 0}; a0[rt] = z; a1[rt] = z; }
#pragma unroll
    for (int s = 0; s < 4; s++)
#pragma unroll
      for (int rt = 0; rt < RT; rt++) {
        u4 A = *(const u4*)(sS + (rt * 16 + c) * HP + (32 * s + 8 * g) * 2);
        a0[rt] = __builtin_amdgcn_mfma_f32_16x16x32_bf16(as_s8(A), as_s8(B[0][s]), a0[rt], 0, 0, 0);
        a1[rt] = __builtin_amdgcn_mfma_f32_16x16x32_bf16(as_s8(A), as_s8(B[1][s]), a1[rt], 0, 0, 0);
      }
#pragma unroll
    for (int rt = 0; rt < RT; rt++)
#pragma unroll
      for (int r = 0; r < 4; r++) *(u32*)(sH + (rt * 16 + 4 * g + r) * HP + (32 * wv + 2 * c) * 2) = pkbf(a0[rt][r], a1[rt][r]);
  }
  bar_lds();
  constexpr int NI = RT * 16 * 64 / 1024;
  static_assert(NI * 1024 == RT * 16 * 64, "items");
#pragma unroll
  for (int k = 0; k < NI; k++) {
    const int i = tid + k * 1024;
    u32* hp = (u32*)(sH + (i >> 6) * HP + (i & 63) * 4);
    const u32 xr = *hp;
    const float x0 = lo16(xr), x1 = hi16(xr);
    const float h0 = x0 * __builtin_amdgcn_rcpf(1.f + __builtin_amdgcn_exp2f(x0 * -1.4426950408889634f));
    const float h1 = x1 * __builtin_amdgcn_rcpf(1.f + __builtin_amdgcn_exp2f(x1 * -1.4426950408889634f));
    *hp = pkbf(h0, h1);
  }
  bar_lds();
  if (wv >= 4) {
    make_b();
    const rsrc_t rp = mk_rsrc(phi, (u32)N * 1024u);
    const u32 oo = (u32)((n0 + 4 * g + (c & 3)) * 1024 + 64 * (wv - 4) + 16 * (c >> 2));
    const bool b0 = c & 1, b1 = c & 2;
#pragma unroll
    for (int rt = 0; rt < RT; rt++) {
      f4 a0 = {0, 0, 0, 0}, a1 = {0, 0, 0, 0};
#pragma unroll
      for (int s = 0; s < 4; s++) {
        u4 A = *(const u4*)(sH + (rt * 16 + c) * HP + (32 * s + 8 * g) * 2);
        a0 = __builtin_amdgcn_mfma_f32_16x16x32_bf16(as_s8(A), as_s8(B[0][s]), a0, 0, 0, 0);
        a1 = __builtin_amdgcn_mfma_f32_16x16x32_bf16(as_s8(A), as_s8(B[1][s]), a1, 0, 0, 0);
      }
      u32 d[4];
#pragma unroll
      for (int r = 0; r < 4; r++) d[r] = pkbf(a0[r], a1[r]);
#pragma unroll
      for (int r = 0; r < 4; r += 2) {
        const u32 t = (u32)__builtin_amdgcn_update_dpp(0, (int)(b0 ? d[r] : d[r + 1]), 0xb1, 0xf, 0xf, true);
        if (b0) d[r] = t; else d[r + 1] = t;
      }
      {
        const u32 t0 = (u32)__builtin_amdgcn_update_dpp(0, (int)(b1 ? d[0] : d[2]), 0x4e, 0xf, 0xf, true);
        const u32 t1 = (u32)__builtin_amdgcn_update_dpp(0, (int)(b1 ? d[1] : d[3]), 0x4e, 0xf, 0xf, true);
        if (b1) { d[0] = t0; d[1] = t1; } else { d[2] = t0; d[3] = t1; }
      }
      u4 o = {d[0], d[1], d[2], d[3]};
      __builtin_amdgcn_raw_buffer_store_b128(o, rp, oo, rt * 16 * 1024, AUX_SC1);
    }
  }
}

template <int NPR, bool VEC>
__global__ __launch_bounds__(1024) void k_prep(const u16* __restrict__ sv, const u16* __restrict__ pos, const int* __restrict__ src,
                                               const int* __restrict__ dst, const u16* __restrict__ w1, const u16* __restrict__ w2,
                                               u16* __restrict__ phi, u4* __restrict__ rec, u4* __restrict__ ovf,
                                               float* __restrict__ split, int N, int E, int P, u32 magicM) {
  extern __shared__ __attribute__((aligned(16))) char lds[];
  if ((int)blockIdx.x < P) filter_role<NPR, VEC>(lds, sv, pos, src, dst, rec, ovf, split, N, E, P, magicM);
  else mlp_role(lds, sv, w1, w2, phi, N, blockIdx.x - P);
}

constexpr int NW = 8;
constexpr int NT = NW * 64;
constexpr int CH = 512;
constexpr int SLOTS = CH + MMAX + 32;
constexpr int KEYN = 24;
constexpr int L_PA = 0;
constexpr int L_PB = L_PA + SLOTS * 16;
constexpr int L_HT = L_PB + SLOTS * 16;
constexpr int L_RAW = L_HT;
constexpr int L_KEY = L_RAW + CH * 16;
constexpr int L_CNT = L_KEY + MMAX * KEYN * 4;
constexpr int L_START = L_CNT + MMAX * 4;
constexpr int L_MISC = L_START + MMAX * 4;
constexpr int L_PEND = L_MISC + 256;
constexpr int L_W = L_PEND + NT * 4;
constexpr int L_TOTAL = L_W + 6 * 32 * 8;
static_assert(L_CNT - L_HT >= 2 * NW * 1024 && L_TOTAL <= 80 * 1024, "LDS layout");

DI f4 mfma444(u2 a, u2 b, f4 c) {
  return __builtin_amdgcn_mfma_f32_4x4x4bf16_1k(__builtin_bit_cast(s4, a), __builtin_bit_cast(s4, b), c, 0, 0, 0);
}

DI void agg_range(char* lds, rsrc_t rphi, rsrc_t rsv, rsrc_t rres, rsrc_t rout, u32 lofs, u32 nb, int lane, int wv, int itb, int ite) {
  const int half = lane >> 5, lp = lane & 31;
  u2 ident;
  ident.x = (lane & 3) == 0 ? 0x00003f80u : ((lane & 3) == 1 ? 0x3f800000u : 0u);
  ident.y = (lane & 3) == 2 ? 0x00003f80u : ((lane & 3) == 3 ? 0x3f800000u : 0u);
  const f4 zero = {0, 0, 0, 0};
  f2 acc[4];
#pragma unroll
  for (int r = 0; r < 4; r++) { f2 z = {0.f, 0.f}; acc[r] = z; }
  const u32 olane = half * 512u + lofs;
  u32 maskw = 0xfffffc00u;
  asm volatile("" : "+s"(maskw));
  const char* pbl = lds + L_PB + half * 16 + itb * 32;
  const char* pal = lds + L_PA + half * 16 + (itb + (lane & 3)) * 32;
  const char* wl = lds + L_W + lp * 8;
  bool pend = false, tail = false;
  if (itb < ite) {
    if (itb > 0) pend = (__builtin_amdgcn_readfirstlane(*(const u32*)(lds + L_PB + (itb - 1) * 32 + 12)) & 1u) == 0;
    tail = (__builtin_amdgcn_readfirstlane(*(const u32*)(lds + L_PB + (ite - 1) * 32 + 12)) & 1u) == 0;
  }
  u32 headso = 0xffffffffu;
  u2 b[4][3]; u32 sw[4], q[4][2];
  auto reduce = [&](f4& o) INL {
#pragma unroll
    for (int r = 0; r < 2; r++) {
      auto x = __builtin_amdgcn_permlane32_swap(__float_as_uint(acc[r].x), __float_as_uint(acc[r + 2].x), false, false);
      auto y = __builtin_amdgcn_permlane32_swap(__float_as_uint(acc[r].y), __float_as_uint(acc[r + 2].y), false, false);
      o[2 * r] = __uint_as_float(x[0]) + __uint_as_float(x[1]);
      o[2 * r + 1] = __uint_as_float(y[0]) + __uint_as_float(y[1]);
    }
  };
  for (int it = itb; it < ite; it += 4, pbl += 128, pal += 128) {
    __builtin_amdgcn_s_setprio(3);
    u32 w[4];
#pragma unroll
    for (int u = 0; u < 4; u++) w[u] = *(const u32*)(pbl + u * 32 + 12);
    const u4 am = *(const u4*)pal;
    u2 wt[3][2];
#pragma unroll
    for (int g = 0; g < 3; g++)
#pragma unroll
      for (int t = 0; t < 2; t++) wt[g][t] = *(const u2*)(wl + (g * 2 + t) * 256);
#pragma unroll
    for (int u = 0; u < 4; u++) {
      sw[u] = __builtin_amdgcn_readfirstlane(w[u]);
      if (u == 0 || it + u < ite) {
        const u32 vo = (w[u] & maskw) | lofs;
        b[u][0].x = bld(rphi, vo, 0);
        b[u][0].y = bld(rphi, vo + 256, 0);
        b[u][1].x = bld(rphi, vo + 512, 0);
        b[u][1].y = bld(rsv, vo + 256, 0);
        b[u][2].x = bld(rsv, vo + 512, 0);
        b[u][2].y = bld(rsv, vo + 768, 0);
        if (sw[u] & 1u) {
          const u32 sq = (nb + ((sw[u] >> 1) & 0x1ffu)) << 10;
          q[u][0] = bld(rres, olane, sq); q[u][1] = bld(rres, olane + 256, sq);
        }
      }
    }
    asm volatile("" ::: "memory");
    __builtin_amdgcn_s_setprio(0);
    f4 wf[3][2];
#pragma unroll
    for (int g = 0; g < 3; g++)
#pragma unroll
      for (int t = 0; t < 2; t++) {
        u2 ah = {am.x, am.y}, al = {am.z, am.w};
        wf[g][t] = mfma444(al, wt[g][t], mfma444(ah, wt[g][t], zero));
      }
    float rr[4][3];
#pragma unroll
    for (int u = 0; u < 4; u++) {
      const u32* rp = (const u32*)(pbl + u * 32);
      rr[u][0] = __uint_as_float(rp[0]); rr[u][1] = __uint_as_float(rp[1]); rr[u][2] = __uint_as_float(rp[2]);
    }
#pragma unroll
    for (int u = 0; u < 4; u++) {
      if (u == 0 || it + u < ite) {
        const f4 U0 = mfma444(ident, b[u][0], zero);
        const f4 U1 = mfma444(ident, b[u][1], zero);
        const f4 U2 = mfma444(ident, b[u][2], zero);
        acc[0].x = __builtin_fmaf(U0[0], wf[0][0][u], acc[0].x);
        acc[0].y = __builtin_fmaf(U0[1], wf[0][1][u], acc[0].y);
        const float mv0 = U0[2] * wf[1][0][u], mv1 = U0[3] * wf[1][1][u];
        const float mr0 = U1[0] * wf[2][0][u], mr1 = U1[1] * wf[2][1][u];
        acc[1].x = __builtin_fmaf(mv0, U1[2], acc[1].x); acc[1].y = __builtin_fmaf(mv1, U1[3], acc[1].y);
        acc[2].x = __builtin_fmaf(mv0, U2[0], acc[2].x); acc[2].y = __builtin_fmaf(mv1, U2[1], acc[2].y);
        acc[3].x = __builtin_fmaf(mv0, U2[2], acc[3].x); acc[3].y = __builtin_fmaf(mv1, U2[3], acc[3].y);
        acc[1].x = __builtin_fmaf(mr0, rr[u][0], acc[1].x); acc[1].y = __builtin_fmaf(mr1, rr[u][0], acc[1].y);
        acc[2].x = __builtin_fmaf(mr0, rr[u][1], acc[2].x); acc[2].y = __builtin_fmaf(mr1, rr[u][1], acc[2].y);
        acc[3].x = __builtin_fmaf(mr0, rr[u][2], acc[3].x); acc[3].y = __builtin_fmaf(mr1, rr[u][2], acc[3].y);
        if (sw[u] & 1u) {
          const u32 so = (nb + ((sw[u] >> 1) & 0x1ffu)) << 10;
          f4 o;
          reduce(o);
          u2 qq = {q[u][0], q[u][1]};
          o = mfma444(ident, qq, o);
          if (pend) {
            *(f4*)(lds + L_HT + (wv * 64 + lane) * 16) = o;
            headso = so; pend = false;
          } else {
            bst(pkbf(o[0], o[1]), rout, olane, so);
            bst(pkbf(o[2], o[3]), rout, olane + 256, so);
          }
#pragma unroll
          for (int r = 0; r < 4; r++) { f2 z = {0.f, 0.f}; acc[r] = z; }
        }
      }
    }
  }
  char* hl = lds + L_HT + (wv * 64 + lane) * 16;
  if (tail) {
    f4 o;
    reduce(o);
    *(f4*)(hl + NW * 1024) = o;
  }
  if (lane == 0) *(u32*)(lds + L_MISC + (32 + wv) * 4) = (tail ? 1u : 0u) | ((pend || itb >= ite) ? 2u : 0u);
  bar_lds();
  if (headso != 0xffffffffu) {
    f4 o = *(const f4*)hl;
    for (int k = wv - 1; k >= 0; k--) {
      const u32 st = __builtin_amdgcn_readfirstlane(*(const u32*)(lds + L_MISC + (32 + k) * 4));
      if (st & 1u) o += *(const f4*)(lds + L_HT + ((NW + k) * 64 + lane) * 16);
      if (!(st & 2u)) break;
    }
    bst(pkbf(o[0], o[1]), rout, olane, headso);
    bst(pkbf(o[2], o[3]), rout, olane + 256, headso);
  }
}

NOPK __global__ __launch_bounds__(NT, 4) void k_agg(const u16* __restrict__ sv, const u16* __restrict__ pos, const u16* __restrict__ freqs,
                                                    const u16* __restrict__ wr, const u16* __restrict__ phi, const u4* __restrict__ rec,
                                                    const u4* __restrict__ ovf, const float* __restrict__ split, u16* out, int N, int P, int M) {
  __shared__ __attribute__((aligned(16))) char lds[L_TOTAL];
  const int tid = threadIdx.x, lane = tid & 63, wv = __builtin_amdgcn_readfirstlane(tid >> 6);
  const int label = blockIdx.x & 7, q = label >> 1, h = label & 1, j = blockIdx.x >> 3;
  const int cell = q * JG + j, nb = j * M;
  u32* cnt = (u32*)(lds + L_CNT);
  u32* start = (u32*)(lds + L_START);
  u32* misc = (u32*)(lds + L_MISC);
  u4* raw = (u4*)(lds + L_RAW);
  u4* pa = (u4*)(lds + L_PA);
  u4* pb = (u4*)(lds + L_PB);
  const char* svb = (const char*)sv;
  constexpr int RND = (PMAX * SLOTK + NT - 1) / NT;

  const u32 nc = min(nb + tid, N - 1);
  const u16* pp = (const u16*)((const char*)pos + nc * 6u);
  const u32 px = pp[0], py = pp[1];
  const float sx = split[0], sy = split[1];
  __builtin_amdgcn_sched_barrier(0);
  u4 pre[RND];
#pragma unroll
  for (int i = 0; i < RND; i++) pre[i] = rec[(u32)cell * (u32)(P * SLOTK) + min(tid + i * NT, P * SLOTK - 1)];
  constexpr int NWE = 6 * 32;
  u32 wk[4];
  {
    const int we = min(tid, NWE - 1), gt = we / 32, l = we % 32;
    const u16* wp = wr + (gt >> 1) * 128 + h * 64 + 2 * l + (gt & 1);
#pragma unroll
    for (int k = 0; k < 4; k++) wk[k] = wp[k * 384];
  }
  const u32 fq01 = *(const u32*)freqs, fq23 = *(const u32*)(freqs + 2);
  __builtin_amdgcn_sched_barrier(0);
  for (int i = tid; i < MMAX; i += NT) cnt[i] = 0;
  for (int i = tid; i < MMAX * KEYN / 4; i += NT) { u4 f = {~0u, ~0u, ~0u, ~0u}; *(u4*)(lds + L_KEY + i * 16) = f; }
  if (tid < 2) misc[tid] = 0;
  bar_lds();

  const bool member = tid < M && nb + tid < N && quadrant(lo16(px), lo16(py), sx, sy) == (u32)q;
  auto stage = [&](u4 t, u32 pos_) INL {
    const u32 rnk = atomicAdd(&cnt[t.y - nb], 1u);
    t.w = (t.w & 0xffffu) | (rnk << 16);
    raw[pos_] = t;
    if (rnk < KEYN) *(u32*)(lds + L_KEY + ((t.y - nb) * KEYN + rnk) * 4) = (t.x << 8) | rnk;
  };
  {
    u32 pendm = 0;
    unsigned long long vm[RND], vm2[RND];
    u4 pre2[RND];
    bool ovfl = false;
#pragma unroll
    for (int i = 0; i < RND; i++) {
      const int sl = tid + i * NT;
      const u32 c = sl < P * SLOTK ? pre[i].w >> 16 : 0u, k = sl % SLOTK;
      const bool v2 = k + SLOTK < c;
      vm[i] = ballot64(k < c);
      vm2[i] = ballot64(v2);
      if (vm2[i]) pre2[i] = ovf[v2 ? (u32)(sl / SLOTK) * CHUNK + (pre[i].y >> 16) + k : 0u];
      pre[i].y &= 0xffffu;
      ovfl |= c > 2 * SLOTK;
    }
    u32 tot = 0;
#pragma unroll
    for (int i = 0; i < RND; i++) tot += __popcll(vm[i]) + __popcll(vm2[i]);
    u32 base = 0;
    if (lane == 0) base = atomicAdd(&misc[0], tot);
    if (ballot64(ovfl) != 0 && lane == 0) misc[1] = 1;
    base = __builtin_amdgcn_readfirstlane(base);
#pragma unroll
    for (int i = 0; i < RND; i++) {
      const u32 pos_ = base + mbcnt64(vm[i]);
      base += __popcll(vm[i]);
      if (vm[i] >> lane & 1) { if (pos_ < CH) stage(pre[i], pos_); else pendm |= 1u << i; }
    }
#pragma unroll
    for (int i = 0; i < RND; i++) {
      if (vm2[i]) {
        const u32 pos_ = base + mbcnt64(vm2[i]);
        base += __popcll(vm2[i]);
        if (vm2[i] >> lane & 1) { if (pos_ < CH) stage(pre2[i], pos_); else pendm |= 1u << (RND + i); }
      }
    }
    *(u32*)(lds + L_PEND + tid * 4) = pendm;
  }
  if (tid < NWE) { u2 t = {wk[0] | (wk[1] << 16), wk[2] | (wk[3] << 16)}; *(u2*)(lds + L_W + tid * 8) = t; }
  const rsrc_t rphi = mk_rsrc(phi, (u32)N * 1024u), rout = mk_rsrc(out, (u32)N * 1024u);
  bar_lds();
  u32 touch[4];
#pragma unroll
  for (int i = 0; i < 4; i++) touch[i] = *(const u32*)(svb + (member ? nc * 1024u + i * 256u + h * 128u : 0u));
  const u32 nslot = __builtin_amdgcn_readfirstlane(misc[0]);
  const bool anyovf = __builtin_amdgcn_readfirstlane(misc[1]) != 0;
  u32 total = nslot;
  auto stage_ovf = [&](u32 c0, int lane_) INL -> u32 {
    u32 pos_ = nslot;
    for (int p0 = 0; p0 < P; p0 += 64) {
      u4 r0 = {0, 0, 0, 0};
      if (p0 + lane_ < P) r0 = rec[(u32)(cell * P + p0 + lane_) * SLOTK];
      const u32 d = r0.y >> 16, c = r0.w >> 16, n = c > 2 * SLOTK ? c - 2 * SLOTK : 0u;
      const u32 inc = wave_scan(n);
      if (wv == 0) {
        const u32 my = pos_ + inc - n;
        for (u32 k = 0; k < n; k++)
          if (my + k >= c0 && my + k < c0 + CH) stage(ovf[(size_t)(p0 + lane_) * CHUNK + d + SLOTK + k], my + k - c0);
      }
      pos_ += __builtin_amdgcn_readlane(inc, 63);
    }
    return pos_;
  };
  if (anyovf) { total = stage_ovf(0u, lane); bar_lds(); }

  auto process = [&](const bool FIRST, u32 nrec, int tid_) INL {
    const int lane = tid_ & 63;
    const u32 lofs = h * 128 + (lane & 31) * 4;
    int T;
    {
      const u4 c4 = *(const u4*)(cnt + lane * MPL);
      u32 it[MPL] = {c4.x, c4.y, c4.z, c4.w}, sum = 0;
#pragma unroll
      for (int i = 0; i < MPL; i++) { it[i] = (it[i] + 1) >> 1; sum += it[i]; }
      const u32 inc = wave_scan(sum);
      T = (int)__builtin_amdgcn_readlane(inc, 63);
      u32 ex = inc - sum;
      u4 s4;
#pragma unroll
      for (int k = 0; k < MPL; k++) { s4[k] = ex; ex += it[k]; }
      *(u4*)(start + lane * MPL) = s4;
    }
    const float frq[4] = {lo16(fq01), hi16(fq01), lo16(fq23), hi16(fq23)};
    for (u32 i0 = wv * 64; i0 < nrec; i0 += NT) {
      const u32 i = i0 + lane;
      const u4 t = raw[min(i, (u32)CH - 1)];
      const u32 loc = min(t.y - nb, (u32)MMAX - 1);
      u32 rnk = t.w >> 16;
      {
        const u32 cc = cnt[loc], mine = (t.x << 8) | rnk;
        const u4* kp = (const u4*)(lds + L_KEY + loc * (KEYN * 4));
        u32 rk = 0;
#pragma unroll
        for (int b = 0; b < KEYN / 8; b++) {
          if (b == 0 || ballot64(cc > 8 * b) != 0) {
            const u4 k0 = kp[2 * b], k1 = kp[2 * b + 1];
#pragma unroll
            for (int k = 0; k < 4; k++) rk += (k0[k] < mine ? 1u : 0u) + (k1[k] < mine ? 1u : 0u);
          }
        }
        if (cc <= KEYN) rnk = rk;
      }
      const float rx = lo16(t.z), ry = hi16(t.z), rz = lo16(t.w);
      const u32 s01 = pkbf(rx * rx, ry * ry);
      float ss = lo16(s01) + hi16(s01) + rbf(rz * rz);
      ss = rbf(ss); ss = rbf(ss + 1e-8f);
      const float dd = rbf(__builtin_amdgcn_sqrtf(ss));
      const float id = __builtin_amdgcn_rcpf(dd);
      const u32 rh01 = pkbf(rx * id, ry * id);
      u4 ca, cb;
      cb.x = rh01 << 16; cb.y = rh01 & 0xffff0000u; cb.z = pkbf(0.f, rz * id) & 0xffff0000u;
      float fc = rbf(3.14159265358979f * dd);
      fc = rbf(fc * 0.2f);
      fc = rbf(__builtin_amdgcn_cosf(__builtin_amdgcn_fractf(fc * 0.15915494309189535f)));
      fc = 0.5f * rbf(fc + 1.0f);
      if (!(dd < 5.0f)) fc = 0.0f * dd;
      float a[4];
#pragma unroll
      for (int k = 0; k < 4; k += 2) {
        const u32 x = pkbf(dd * frq[k], dd * frq[k + 1]);
        const u32 y = pkbf(__builtin_amdgcn_sinf(__builtin_amdgcn_fractf(lo16(x) * 0.15915494309189535f)),
                           __builtin_amdgcn_sinf(__builtin_amdgcn_fractf(hi16(x) * 0.15915494309189535f)));
        const u32 z = pkbf(lo16(y) * id, hi16(y) * id);
        a[k] = lo16(z) * fc; a[k + 1] = hi16(z) * fc;
      }
      ca.x = pkbf(a[0], a[1]); ca.y = pkbf(a[2], a[3]);
      ca.z = pkbf(a[0] - lo16(ca.x), a[1] - hi16(ca.x)); ca.w = pkbf(a[2] - lo16(ca.y), a[3] - hi16(ca.y));
      if (i < nrec) {
        const u32 st = start[loc], cc = cnt[loc], sl = 2 * st + rnk;
        const u32 last = (rnk | 1u) + 1 >= cc;
        cb.w = (t.x << 10) | (loc << 1) | last;
        pa[sl] = ca; pb[sl] = cb;
        if (rnk + 1 == cc && (cc & 1)) { u4 z = {0, 0, 0, 0}; pa[sl + 1] = z; u4 t2 = {0, 0, 0, cb.w}; pb[sl + 1] = t2; }
      }
    }
    if (tid_ < 32) { u4 z = {0, 0, 0, 0}; pa[2 * T + tid_] = z; pb[2 * T + tid_] = z; }
    bar_lds();
    const int itb = (wv * T) / NW, ite = ((wv + 1) * T) / NW;
#pragma unroll
    for (int i = 0; i < 4; i++) asm volatile("" ::"v"(touch[i]));
    agg_range(lds, rphi, mk_rsrc(sv, (u32)N * 1024u), mk_rsrc(FIRST ? sv : (const u16*)out, (u32)N * 1024u), rout, lofs, nb, lane, wv, itb, ite);
  };
  unsigned long long em = ballot64(member && cnt[min(tid, MMAX - 1)] == 0);
  u32 left = nslot > CH ? nslot - CH : 0u;
  u32 c0 = max(nslot, (u32)CH);
  u32 nrec = min(total, (u32)CH);
  bool first = true;
  for (;;) {
    int tz = threadIdx.x;
    asm volatile("" : "+v"(tz));
    process(first, nrec, tz);
    asm volatile("" : "+v"(tz));
    const int lz = tz & 63;
    if (em) {
      const u32 ol = (lz >> 5) * 512u + h * 128 + (lz & 31) * 4;
      const rsrc_t rs = mk_rsrc(sv, (u32)N * 1024u);
      while (em) {
        u32 so[4], v[4][2];
#pragma unroll
        for (int k = 0; k < 4; k++) {
          so[k] = em ? (u32)(nb + wv * 64 + __builtin_ctzll(em)) << 10 : OOB;
          em &= em - 1;
          v[k][0] = bld(rs, ol, so[k]); v[k][1] = bld(rs, ol + 256, so[k]);
        }
#pragma unroll
        for (int k = 0; k < 4; k++) { bst(v[k][0], rout, ol, so[k]); bst(v[k][1], rout, ol + 256, so[k]); }
      }
    }
    if (left == 0 && c0 >= total) break;
    first = false;
    bar_all();
    for (int i = tz; i < MMAX; i += NT) cnt[i] = 0;
    for (int i = tz; i < MMAX * KEYN / 4; i += NT) { u4 f = {~0u, ~0u, ~0u, ~0u}; *(u4*)(lds + L_KEY + i * 16) = f; }
    if (tz == 0) misc[0] = 0;
    bar_lds();
    if (left) {
      u32 pendm = *(const u32*)(lds + L_PEND + tz * 4);
#pragma unroll
      for (int i = 0; i < 2 * RND; i++) {
        const unsigned long long m = ballot64(pendm >> i & 1);
        if (m) {
          u32 base = 0;
          if (lz == 0) base = atomicAdd(&misc[0], (u32)__popcll(m));
          base = __builtin_amdgcn_readfirstlane(base);
          const u32 pos_ = base + mbcnt64(m);
          if ((pendm >> i & 1) && pos_ < CH) {
            const int sl = tz + (i % RND) * NT;
            u4 r0 = rec[(u32)cell * (u32)(P * SLOTK) + sl];
            if (i < RND) { r0.y &= 0xffffu; stage(r0, pos_); }
            else stage(ovf[(u32)(sl / SLOTK) * CHUNK + (r0.y >> 16) + sl % SLOTK], pos_);
            pendm &= ~(1u << i);
          }
        }
      }
      *(u32*)(lds + L_PEND + tz * 4) = pendm;
      nrec = min(left, (u32)CH);
      left -= nrec;
    } else {
      stage_ovf(c0, lz);
      nrec = min(total - c0, (u32)CH);
      c0 += CH;
    }
    bar_lds();
  }
}

__global__ void fb_mlp(const u16* sv, const u16* w1, const u16* w2, u16* phi) {
  __shared__ float s[128], hh[128];
  int n = blockIdx.x, t = threadIdx.x;
  s[t] = lo16(sv[(size_t)n * 512 + t]);
  __syncthreads();
  float acc = 0.f;
  for (int k = 0; k < 128; k++) acc += s[k] * lo16(w1[k * 128 + t]);
  acc = rbf(acc);
  hh[t] = rbf(acc / (1.f + expf(-acc)));
  __syncthreads();
  for (int g = 0; g < 3; g++) {
    float a = 0.f;
    for (int k = 0; k < 128; k++) a += hh[k] * lo16(w2[k * 384 + g * 128 + t]);
    phi[(size_t)n * 384 + g * 128 + t] = (u16)pkbf(a, 0.f);
  }
}
__global__ void fb_edge(const u16* sv, const u16* pos, const int* src, const int* dst, const u16* freqs, const u16* wr,
                        const u16* phi, float* acc) {
  int e = blockIdx.x, t = threadIdx.x;
  size_t s = src[e], d = dst[e];
  float r[3], ss = 0.f;
  for (int x = 0; x < 3; x++) { r[x] = rbf(lo16(pos[s * 3 + x]) - lo16(pos[d * 3 + x])); ss += rbf(r[x] * r[x]); }
  ss = rbf(ss); ss = rbf(ss + 1e-8f);
  float dd = rbf(sqrtf(ss));
  if (dd >= 5.0f && dd <= 3.0e38f) return;
  float fc = rbf(3.14159265358979f * dd); fc = rbf(fc / 5.0f); fc = rbf(cosf(fc)); fc = rbf(fc + 1.0f); fc = rbf(0.5f * fc);
  if (!(dd < 5.0f)) fc = 0.0f * dd;
  float wf[3] = {0.f, 0.f, 0.f};
  for (int k = 0; k < 4; k++) {
    float a = rbf(rbf(sinf(rbf(dd * lo16(freqs[k])))) / dd) * fc;
    for (int g = 0; g < 3; g++) wf[g] += a * lo16(wr[k * 384 + g * 128 + t]);
  }
  atomicAdd(&acc[d * 512 + t], lo16(phi[s * 384 + t]) * wf[0]);
  float mv = lo16(phi[s * 384 + 128 + t]) * wf[1], mr = lo16(phi[s * 384 + 256 + t]) * wf[2];
  for (int x = 0; x < 3; x++) atomicAdd(&acc[d * 512 + 128 + x * 128 + t], mv * lo16(sv[s * 512 + 128 + x * 128 + t]) + mr * rbf(r[x] / dd));
}
__global__ void fb_fin(const u16* sv, const float* acc, u16* out, size_t n) {
  size_t i = blockIdx.x * (size_t)256 + threadIdx.x;
  if (i < n) out[i] = (u16)pkbf(lo16(sv[i]) + acc[i], 0.f);
}

extern "C" void kernel_launch(void* const* d_in, const int* in_sizes, int n_in, void* d_out, int out_size, void* d_ws,
                              size_t ws_size, hipStream_t stream) {
  const u16* sv = (const u16*)d_in[0];
  const u16* pos = (const u16*)d_in[1];
  const int* src = (const int*)d_in[2];
  const int* dst = (const int*)d_in[3];
  const u16* freqs = (const u16*)d_in[4];
  const u16* w1 = (const u16*)d_in[5];
  const u16* w2 = (const u16*)d_in[6];
  const u16* wr = (const u16*)d_in[7];
  u16* out = (u16*)d_out;
  const int N = in_sizes[0] / 512, E = in_sizes[2];
  const int P = (E + CHUNK - 1) / CHUNK;
  const int M = (N + JG - 1) / JG;
  char* ws = (char*)d_ws;
  u16* phi = (u16*)ws;
  size_t off = ((size_t)N * 1024 + 255) & ~(size_t)255;
  if (M >= 2 && N <= 15000 && E >= 4 && M <= MMAX && P <= PMAX) {
    u4* rec = (u4*)(ws + off); off += (size_t)NCELL * P * SLOTK * 16;
    u4* ovf = (u4*)(ws + off); off += (size_t)P * CHUNK * 16;
    float* split = (float*)(ws + off);
    const u32 magicM = (u32)(0x100000000ull / (u32)M) + 1u;
    const int nmlp = (N + RT * 16 - 1) / (RT * 16);
    size_t lds1 = (size_t)((N + 1) & ~1) * 8 + NCELL * 8 + 256 + NCELL * SLOTK * 16;
    const size_t ldsm = 2 * RT * 16 * HP;
    if (lds1 < ldsm) lds1 = ldsm;
#define LAUNCH_PREP(NPR_, VEC_) do { \
      (void)hipFuncSetAttribute((const void*)k_prep<NPR_, VEC_>, hipFuncAttributeMaxDynamicSharedMemorySize, 160 * 1024); \
      k_prep<NPR_, VEC_><<<P + nmlp, 1024, lds1, stream>>>(sv, pos, src, dst, w1, w2, phi, rec, ovf, split, N, E, P, magicM); } while (0)
    if (N <= 7 * 2048 && (E & 3) == 0) LAUNCH_PREP(7, true);
    else if ((E & 3) == 0) LAUNCH_PREP(8, true);
    else LAUNCH_PREP(8, false);
    k_agg<<<8 * JG, NT, 0, stream>>>(sv, pos, freqs, wr, phi, rec, ovf, split, out, N, P, M);
  } else {
    float* acc = (float*)(ws + off);
    (void)hipMemsetAsync(acc, 0, (size_t)N * 512 * 4, stream);
    fb_mlp<<<N, 128, 0, stream>>>(sv, w1, w2, phi);
    if (E > 0) fb_edge<<<E, 128, 0, stream>>>(sv, pos, src, dst, freqs, wr, phi, acc);
    fb_fin<<<(unsigned)(((size_t)N * 512 + 255) / 256), 256, 0, stream>>>(sv, acc, out, (size_t)N * 512);
  }
}
```
